```python
import torch

def _softmax(x, axis=-1):
    return torch.softmax(x, dim=axis)
def _gelu(x):
    return torch.nn.functional.gelu(x, approximate="tanh")
def _silu(x):
    return torch.nn.functional.silu(x)
def _sigmoid(x):
    return torch.sigmoid(x)
def _layernorm(x, axis=-1, eps=1e-5):
    m = x.mean(dim=axis, keepdim=True)
    v = x.var(dim=axis, keepdim=True, unbiased=False)
    return (x - m) / torch.sqrt(v + eps)
def _scatter_add(tgt, idx, src, axis=0):
    return tgt.clone().index_add_(axis, idx, src)
def _segment_sum(src, seg, n):
    out = torch.zeros((n,) + src.shape[1:], dtype=src.dtype)
    return out.index_add_(0, seg, src)
def _pad(x, pw):
    flat = tuple(v for pair in reversed(pw) for v in pair)
    return torch.nn.functional.pad(x, flat)

kernel_name = 'gated_mix__softmax_full_mha__glu_gated_bilinear__prompt'

def setup_inputs():

    _w = (lambda a: (lambda t: t.to(torch.bfloat16) if t.is_floating_point() else t)(torch.as_tensor(a)))
    def _A_setup():
        B, S, D = 1, 2048, 512
        return {
            "x": _w((lambda *s: torch.randn(*s, dtype=torch.bfloat16) * 0.05)(B, S, D)),
            "wqkv": _w((lambda *s: torch.randn(*s, dtype=torch.bfloat16) * 0.8)(D, 3 * D)),
            "wo": _w((lambda *s: torch.randn(*s, dtype=torch.bfloat16) * 0.05)(D, D)),
        }
    _A_in = _A_setup()

    def _B_setup():
        B, S, D, F = 1, 2048, 512, 1792
        return {
            "x": _w((lambda *s: torch.randn(*s, dtype=torch.bfloat16) * 0.05)(B, S, D)),
            "w_gate": _w((lambda *s: torch.randn(*s, dtype=torch.bfloat16) * 1.6)(D, F) * (D ** -0.5)),
            "w_up": _w((lambda *s: torch.randn(*s, dtype=torch.bfloat16) * 1.6)(D, F) * (D ** -0.5)),
            "w_down": _w((lambda *s: torch.randn(*s, dtype=torch.bfloat16) * 1.6)(F, D) * (F ** -0.5)),
        }
    _B_in = _B_setup()

    return {
        "A_x": _A_in["x"],
        "A_wqkv": _A_in["wqkv"],
        "A_wo": _A_in["wo"],
        "B_x": _B_in["x"],
        "B_w_gate": _B_in["w_gate"],
        "B_w_up": _B_in["w_up"],
        "B_w_down": _B_in["w_down"],
        "gate_w": _w((lambda *s: torch.randn(*s, dtype=torch.bfloat16) * 0.05)(512) * 16.0),
    }

def reference(A_x, A_wqkv, A_wo, B_x, B_w_gate, B_w_up, B_w_down, gate_w):
    def _A_ref(x, wqkv, wo):
        B, S, D = x.shape
        H = 32; Dh = D // H
        qkv = (lambda x, *d: x.permute(*d))((x @ wqkv).reshape(B, S, 3, H, Dh), 2, 0, 3, 1, 4)
        q, k, v = qkv[0], qkv[1], qkv[2]
        scale = Dh ** -0.5
        scores = torch.einsum("bhqd,bhkd->bhqk", q, k) * scale
        p = _softmax(scores, axis=-1)
        o = torch.einsum("bhqk,bhkd->bhqd", p, v)
        return (lambda x, *d: x.permute(*d))(o, 0, 2, 1, 3).reshape(B, S, D) @ wo

    def _B_ref(x, w_gate, w_up, w_down):
        g = torch.einsum("bsd,df->bsf", x, w_gate)
        u = torch.einsum("bsd,df->bsf", x, w_up)
        h = g * u
        return torch.einsum("bsf,fd->bsd", h, w_down)

    x = A_x
    g = _sigmoid((x * gate_w).sum(-1, keepdims=True))
    a = _A_ref(x, A_wqkv, A_wo)
    b = _B_ref(x, B_w_gate, B_w_up, B_w_down)
    return g * a + (1 - g) * b
```

```cpp
#include <hip/hip_runtime.h>
#include <hip/hip_bf16.h>
typedef __hip_bfloat16 bf16;
#define S 2048
#define D 512
#define F 1792
__device__ inline float b2f(bf16 x){return __bfloat162float(x);}
template<typename TA>
__global__ void gemm_naive(const TA* A, const bf16* B, float* C, int M,int N,int K){
  int n=blockIdx.x*64+(threadIdx.x&63); int m=blockIdx.y*4+(threadIdx.x>>6);
  float acc=0; for(int k=0;k<K;k++) acc+= (float)A[(size_t)m*K+k]*b2f(B[(size_t)k*N+n]);
  C[(size_t)m*N+n]=acc;
}
__global__ void attn_naive(const float* qkv, float* o){
  int q=blockIdx.x*64+(threadIdx.x&63); int h=blockIdx.y*4+(threadIdx.x>>6);
  float qv[16];
#pragma unroll
  for(int d=0;d<16;d++) qv[d]=qkv[(size_t)q*1536+h*16+d]*0.25f;
  float m=-1e30f,l=0,acc[16];
#pragma unroll
  for(int d=0;d<16;d++) acc[d]=0;
  for(int k=0;k<S;k++){
    float s=0;
#pragma unroll
    for(int d=0;d<16;d++) s+=qv[d]*qkv[(size_t)k*1536+512+h*16+d];
    float mn=fmaxf(m,s); float a=__expf(m-mn); float p=__expf(s-mn); m=mn; l=l*a+p;
#pragma unroll
    for(int d=0;d<16;d++) acc[d]=acc[d]*a+p*qkv[(size_t)k*1536+1024+h*16+d];
  }
#pragma unroll
  for(int d=0;d<16;d++) o[(size_t)q*512+h*16+d]=acc[d]/l;
}
__global__ void mul_k(float* g,const float* u,int n){int i=blockIdx.x*256+threadIdx.x; if(i<n) g[i]*=u[i];}
__global__ void fin_k(const bf16* x,const bf16* gw,const float* a,const float* b,bf16* out){
  int t=blockIdx.x; __shared__ float red[256];
  float s=0; for(int i=threadIdx.x;i<D;i+=256) s+=b2f(x[t*D+i])*b2f(gw[i]);
  red[threadIdx.x]=s; __syncthreads();
  for(int o=128;o>0;o>>=1){ if(threadIdx.x<o) red[threadIdx.x]+=red[threadIdx.x+o]; __syncthreads(); }
  float g=1.f/(1.f+__expf(-red[0]));
  for(int i=threadIdx.x;i<D;i+=256) out[t*D+i]=__float2bfloat16(g*a[t*D+i]+(1.f-g)*b[t*D+i]);
}
extern "C" void kernel_launch(void* const* d_in, const int* in_sizes, int n_in,
                              void* d_out, int out_size, void* d_ws, size_t ws_size,
                              hipStream_t stream) {
  const bf16* x=(const bf16*)d_in[0]; const bf16* wqkv=(const bf16*)d_in[1]; const bf16* wo=(const bf16*)d_in[2];
  const bf16* wg=(const bf16*)d_in[4]; const bf16* wu=(const bf16*)d_in[5]; const bf16* wd=(const bf16*)d_in[6]; const bf16* gw=(const bf16*)d_in[7];
  float* ws=(float*)d_ws;
  float* qkv=ws; float* o=qkv+S*1536; float* g=o+S*D; float* u=g+S*F; float* a=u+S*F; float* b=a+S*D;
  gemm_naive<bf16><<<dim3(1536/64,S/4),256,0,stream>>>(x,wqkv,qkv,S,1536,D);
  attn_naive<<<dim3(S/64,8),256,0,stream>>>(qkv,o);
  gemm_naive<float><<<dim3(D/64,S/4),256,0,stream>>>(o,wo,a,S,D,D);
  gemm_naive<bf16><<<dim3(F/64,S/4),256,0,stream>>>(x,wg,g,S,F,D);
  gemm_naive<bf16><<<dim3(F/64,S/4),256,0,stream>>>(x,wu,u,S,F,D);
  mul_k<<<S*F/256,256,0,stream>>>(g,u,S*F);
  gemm_naive<float><<<dim3(D/64,S/4),256,0,stream>>>(g,wd,b,S,D,F);
  fin_k<<<S,256,0,stream>>>(x,gw,a,b,(bf16*)d_out);
}
```

```cpp
#include <hip/hip_runtime.h>
#include <hip/hip_bf16.h>

__global__ void __launch_bounds__(512, 2) gated_mix_fused_kernel(const void* x, const void* wqkv, const void* wo, const void* wg,
    const void* wu, const void* wd, const void* gw, void* out, void* ws) {
  __shared__ __attribute__((aligned(16))) char lds[163840];
  auto kargs = __builtin_amdgcn_kernarg_segment_ptr();
  unsigned ldsbase = (unsigned)(size_t)lds;
  asm volatile(
    "s_getpc_b64 s[50:51]\n"
    "Lstart_%=:\n"
    "s_nop 0\n"
    "s_load_dwordx16 s[4:19], %0, 0x0\n"
    "s_load_dwordx2 s[20:21], %0, 0x40\n"
    "v_lshrrev_b32 v10, 6, %3\n"
    "v_and_b32 v7, 63, %3\n"
    "s_and_b32 s79, %2, 7\n"
    "s_lshr_b32 s68, %2, 3\n"
    "v_readfirstlane_b32 s52, v10\n"
    "s_and_b32 s61, s79, 1\n"
    "s_lshl_b32 s61, s61, 3\n"
    "s_and_b32 s62, s68, 7\n"
    "s_add_u32 s66, s61, s62\n"
    "s_lshr_b32 s63, s79, 1\n"
    "s_lshr_b32 s64, s68, 3\n"
    "s_lshl_b32 s61, s63, 2\n"
    "s_add_u32 s53, s61, s64\n"
    "s_lshr_b32 s61, s79, 1\n"
    "s_xor_b32 s61, s61, s79\n"
    "s_and_b32 s61, s61, 1\n"
    "s_lshl_b32 s54, s61, 2\n"
    "s_lshl_b32 s55, s52, 10\n"
    "s_lshl_b32 s56, s52, 11\n"
    "s_lshl_b32 s61, s68, 3\n"
    "s_add_u32 s61, s61, s52\n"
    "s_lshl_b32 s61, s61, 1\n"
    "v_and_b32 v10, 1, v7\n"
    "v_add_u32 v10, s61, v10\n"
    "v_lshlrev_b32 v10, 7, v10\n"
    "v_min_u32 v10, Lend_%=-Lstart_%=-4, v10\n"
    "global_load_dword v8, v10, s[50:51]\n"
    "v_lshrrev_b32 v10, 3, v7\n"
    "v_lshrrev_b32 v11, 4, v7\n"
    "v_and_b32 v12, 7, v7\n"
    "s_lshl_b32 s61, s66, 7\n"
    "s_lshl_b32 s62, s52, 4\n"
    "s_add_u32 s61, s61, s62\n"
    "v_add_u32 v13, s61, v10\n"
    "v_lshlrev_b32 v13, 10, v13\n"
    "v_xor_b32 v2, v12, v11\n"
    "v_lshl_add_u32 v2, v2, 4, v13\n"
    "v_xor_b32 v3, 64, v2\n"
    "v_add_u32 v3, 0x2000, v3\n"
    "s_mul_i32 s61, s63, 3\n"
    "s_add_u32 s71, s61, s64\n"
    "s_sub_u32 s71, s71, 1\n"
    "s_lshl_b32 s61, s71, 2\n"
    "s_add_u32 s61, s61, 3\n"
    "s_add_u32 s62, s71, 12\n"
    "s_cmp_lt_u32 s71, 4\n"
    "s_cselect_b32 s62, s61, s62\n"
    "s_mov_b32 s61, s53\n"
    "s_add_u32 s71, s71, 9\n"
    "s_mul_i32 s65, s71, 11\n"
    "s_lshr_b32 s65, s65, 5\n"
    "s_add_u32 s71, s71, s65\n"
    "s_mov_b32 s65, s71\n"
    "s_cmp_lg_u32 s64, 0\n"
    "s_cselect_b32 s67, 1, 0\n"
    "s_cselect_b32 s70, s62, s61\n"
    "s_cselect_b32 s60, 7, 6\n"
    "s_cselect_b32 s62, 0, 1\n"
    "s_cselect_b32 s65, s65, s61\n"
    "s_add_u32 s65, s65, s62\n"
    "s_lshl_b32 s38, s60, 18\n"
    "s_mov_b32 s39, 0x20000\n"
    "s_mov_b32 s42, s38\n"
    "s_mov_b32 s43, 0x20000\n"
    "s_lshl_b32 s63, s60, 9\n"
    "s_lshl_b32 s60, s60, 15\n"
    "s_lshl_b32 s61, s52, 3\n"
    "v_add_u32 v10, s61, v10\n"
    "s_and_b32 s61, s52, 1\n"
    "s_lshl_b32 s61, s61, 1\n"
    "v_and_b32 v11, 1, v11\n"
    "v_or_b32 v11, s61, v11\n"
    "v_lshlrev_b32 v11, 1, v11\n"
    "v_xor_b32 v12, v12, v11\n"
    "v_lshlrev_b32 v12, 4, v12\n"
    "v_mul_u32_u24 v13, 3072, v10\n"
    "s_lshl_b32 s61, s70, 7\n"
    "v_add3_u32 v4, v13, v12, s61\n"
    "v_mul_u32_u24 v13, s63, v10\n"
    "s_lshl_b32 s61, s65, 7\n"
    "v_add3_u32 v5, v13, v12, s61\n"
    "s_lshl_b32 s62, s62, 7\n"
    "v_add_u32 v6, s62, v5\n"
    "v_lshlrev_b32 v1, 4, v7\n"
    "s_mov_b32 s26, 2097152\n"
    "s_mov_b32 s27, 0x20000\n"
    "s_mov_b32 s30, 1572864\n"
    "s_mov_b32 s31, 0x20000\n"
    "s_mov_b32 s46, 16777216\n"
    "s_mov_b32 s47, 0x20000\n"
    "s_lshl_b32 s57, s54, 7\n"
    "s_mul_i32 s58, s54, 196608\n"
    "s_mul_i32 s59, s54, s60\n"
    "s_add_u32 s54, s54, 1\n"
    "s_and_b32 s54, s54, 7\n"
    "s_waitcnt lgkmcnt(0)\n"
    "s_mov_b64 s[24:25], s[4:5]\n"
    "s_mov_b64 s[28:29], s[6:7]\n"
    "s_cmp_lg_u32 s67, 0\n"
    "s_cselect_b64 s[36:37], s[10:11], s[6:7]\n"
    "s_cselect_b64 s[40:41], s[12:13], s[6:7]\n"
    "s_add_u32 m0, s56, 0\n"
    "s_nop 0\n"
    "buffer_load_dwordx4 v2, s[24:27], s57 offen lds\n"
    "s_add_u32 m0, s56, 1024\n"
    "s_nop 0\n"
    "buffer_load_dwordx4 v3, s[24:27], s57 offen lds\n"
    "s_add_u32 m0, s55, 16384\n"
    "s_nop 0\n"
    "buffer_load_dwordx4 v4, s[28:31], s58 offen lds\n"
    "s_add_u32 m0, s55, 24576\n"
    "s_nop 0\n"
    "buffer_load_dwordx4 v5, s[36:39], s59 offen lds\n"
    "s_add_u32 m0, s55, 32768\n"
    "s_nop 0\n"
    "buffer_load_dwordx4 v6, s[40:43], s59 offen lds\n"
    "s_mov_b64 s[44:45], s[20:21]\n"
    "s_and_b32 s63, s52, 3\n"
    "s_lshr_b32 s4, s52, 2\n"
    "s_lshl_b32 s61, s66, 2\n"
    "s_add_u32 s5, s61, s63\n"
    "v_and_b32 v10, 15, v7\n"
    "v_lshrrev_b32 v11, 4, v7\n"
    "v_lshrrev_b32 v12, 1, v10\n"
    "v_xor_b32 v12, v12, v11\n"
    "s_lshl_b32 s61, s63, 5\n"
    "v_add_u32 v10, s61, v10\n"
    "v_lshlrev_b32 v10, 7, v10\n"
    "v_lshl_add_u32 v16, v12, 4, v10\n"
    "v_xor_b32 v17, 64, v16\n"
    "v_bfe_u32 v10, v7, 2, 2\n"
    "v_lshrrev_b32 v11, 4, v7\n"
    "v_and_b32 v13, 3, v7\n"
    "v_lshlrev_b32 v13, 3, v13\n"
    "v_lshl_add_u32 v12, v11, 3, v10\n"
    "v_lshl_add_u32 v13, v12, 7, v13\n"
    "v_lshrrev_b32 v10, 1, v10\n"
    "v_and_b32 v11, 1, v11\n"
    "v_lshl_or_b32 v10, v11, 1, v10\n"
    "v_lshl_or_b32 v13, v10, 5, v13\n"
    "s_cmp_lg_u32 s4, 0\n"
    "s_mov_b32 s61, 0x1ac5a928\n"
    "s_cselect_b32 s61, 0x2728c1ee, s61\n"
    "s_mov_b32 s62, 0x22d83128\n"
    "s_cselect_b32 s62, 0x26f9396a, s62\n"
    "s_cmp_lg_u32 s67, 0\n"
    "s_cselect_b32 s61, s62, s61\n"
    "s_bfe_u32 s62, s61, 0x50000\n"
    "s_mul_i32 s62, s62, 0x820\n"
    "s_and_b32 s62, s62, 0xe060\n"
    "v_xor_b32 v18, s62, v13\n"
    "s_bfe_u32 s62, s61, 0x50005\n"
    "s_mul_i32 s62, s62, 0x820\n"
    "s_and_b32 s62, s62, 0xe060\n"
    "v_xor_b32 v19, s62, v13\n"
    "s_bfe_u32 s62, s61, 0x5000a\n"
    "s_mul_i32 s62, s62, 0x820\n"
    "s_and_b32 s62, s62, 0xe060\n"
    "v_xor_b32 v20, s62, v13\n"
    "s_bfe_u32 s62, s61, 0x5000f\n"
    "s_mul_i32 s62, s62, 0x820\n"
    "s_and_b32 s62, s62, 0xe060\n"
    "v_xor_b32 v21, s62, v13\n"
    "s_bfe_u32 s62, s61, 0x50014\n"
    "s_mul_i32 s62, s62, 0x820\n"
    "s_and_b32 s62, s62, 0xe060\n"
    "v_xor_b32 v22, s62, v13\n"
    "s_bfe_u32 s62, s61, 0x50019\n"
    "s_mul_i32 s62, s62, 0x820\n"
    "s_and_b32 s62, s62, 0xe060\n"
    "v_xor_b32 v23, s62, v13\n"
    "s_barrier\n"
    "s_lshl_b32 s57, s54, 7\n"
    "s_mul_i32 s58, s54, 196608\n"
    "s_mul_i32 s59, s54, s60\n"
    "s_add_u32 s54, s54, 1\n"
    "s_and_b32 s54, s54, 7\n"
    "s_add_u32 m0, s56, 40960\n"
    "s_nop 0\n"
    "buffer_load_dwordx4 v2, s[24:27], s57 offen lds\n"
    "s_add_u32 m0, s56, 41984\n"
    "s_nop 0\n"
    "buffer_load_dwordx4 v3, s[24:27], s57 offen lds\n"
    "s_add_u32 m0, s55, 57344\n"
    "s_nop 0\n"
    "buffer_load_dwordx4 v4, s[28:31], s58 offen lds\n"
    "s_add_u32 m0, s55, 65536\n"
    "s_nop 0\n"
    "buffer_load_dwordx4 v5, s[36:39], s59 offen lds\n"
    "s_add_u32 m0, s55, 73728\n"
    "s_nop 0\n"
    "buffer_load_dwordx4 v6, s[40:43], s59 offen lds\n"
    "s_mov_b32 s34, 0xffff\n"
    "s_mov_b32 s35, 0xffff\n"
    "s_cmp_ge_u32 s70, 16\n"
    "s_cbranch_scc1 Lvtype_1_%=\n"
    "s_waitcnt vmcnt(5)\n"
    "s_barrier\n"
    "ds_read_b128 v[24:27], v16 offset:0\n"
    "ds_read_b128 v[28:31], v16 offset:2048\n"
    "ds_read_b64_tr_b16 v[200:201], v18 offset:0\n"
    "ds_read_b64_tr_b16 v[202:203], v18 offset:512\n"
    "ds_read_b64_tr_b16 v[204:205], v19 offset:0\n"
    "ds_read_b64_tr_b16 v[206:207], v19 offset:512\n"
    "ds_read_b64_tr_b16 v[208:209], v20 offset:0\n"
    "ds_read_b64_tr_b16 v[210:211], v20 offset:512\n"
    "ds_read_b64_tr_b16 v[212:213], v21 offset:0\n"
    "ds_read_b64_tr_b16 v[214:215], v21 offset:512\n"
    "ds_read_b64_tr_b16 v[216:217], v22 offset:0\n"
    "ds_read_b64_tr_b16 v[218:219], v22 offset:512\n"
    "ds_read_b64_tr_b16 v[220:221], v23 offset:0\n"
    "ds_read_b64_tr_b16 v[222:223], v23 offset:512\n"
    "s_lshl_b32 s61, s66, 7\n"
    "s_lshl_b32 s62, s53, 3\n"
    "s_add_u32 s61, s61, s62\n"
    "s_add_u32 s69, s61, s52\n"
    "s_lshl_b32 s61, s69, 10\n"
    "s_lshl_b32 s69, s69, 2\n"
    "s_add_u32 s69, s69, 40960\n"
    "buffer_load_dwordx4 v[136:139], v1, s[24:27], s61 offen\n"
    "global_load_dwordx4 v[140:143], v1, s[16:17]\n"
    "buffer_load_dwordx2 v[148:149], off, s[44:47], 0 offset:0 sc1\n"
    "s_waitcnt lgkmcnt(10)\n"
    "v_mfma_f32_16x16x32_bf16 v[152:155], v[200:203], v[24:27], 0\n"
    "ds_read_b128 v[32:35], v17 offset:0\n"
    "ds_read_b128 v[36:39], v17 offset:2048\n"
    "v_mfma_f32_16x16x32_bf16 v[156:159], v[200:203], v[28:31], 0\n"
    "ds_read_b64_tr_b16 v[224:225], v18 offset:4096\n"
    "ds_read_b64_tr_b16 v[226:227], v18 offset:4608\n"
    "s_lshl_b32 s57, s54, 7\n"
    "s_mul_i32 s58, s54, 196608\n"
    "s_mul_i32 s59, s54, s60\n"
    "s_add_u32 s54, s54, 1\n"
    "s_and_b32 s54, s54, 7\n"
    "s_mul_i32 s61, s53, 11\n"
    "s_lshr_b32 s61, s61, 5\n"
    "s_add_u32 s61, s61, s53\n"
    "s_lshl_b32 s62, s53, 2\n"
    "s_sub_u32 s62, s62, 33\n"
    "s_cmp_lt_u32 s53, 9\n"
    "s_cselect_b32 s20, s61, s62\n"
    "s_mul_i32 s61, s5, 112\n"
    "s_lshl_b32 s62, s71, 1\n"
    "s_add_u32 s61, s61, s62\n"
    "s_add_u32 s61, s61, s4\n"
    "s_lshl_b32 s61, s61, 10\n"
    "s_add_u32 s72, s61, 9437184\n"
    "s_waitcnt lgkmcnt(12)\n"
    "v_mfma_f32_16x16x32_bf16 v[164:167], v[204:207], v[28:31], 0\n"
    "ds_read_b64_tr_b16 v[228:229], v19 offset:4096\n"
    "ds_read_b64_tr_b16 v[230:231], v19 offset:4608\n"
    "v_mfma_f32_16x16x32_bf16 v[160:163], v[204:207], v[24:27], 0\n"
    "s_add_u32 m0, s56, 81920\n"
    "ds_read_b64_tr_b16 v[232:233], v20 offset:4096\n"
    "buffer_load_dwordx4 v2, s[24:27], s57 offen lds\n"
    "s_mul_i32 s61, s5, 112\n"
    "s_lshl_b32 s62, s20, 1\n"
    "s_add_u32 s61, s61, s62\n"
    "s_add_u32 s61, s61, s4\n"
    "s_lshl_b32 s61, s61, 10\n"
    "s_add_u32 s81, s61, 9437184\n"
    "s_lshl_b32 s62, s4, 6\n"
    "v_xor_b32 v252, s62, v13\n"
    "s_cmp_lg_u32 s67, 0\n"
    "s_cselect_b32 s61, 2, 6\n"
    "s_mul_i32 s61, s61, s4\n"
    "s_lshl_b32 s62, s70, 2\n"
    "s_add_u32 s61, s61, s62\n"
    "s_waitcnt lgkmcnt(13)\n"
    "v_mfma_f32_16x16x32_bf16 v[168:171], v[208:211], v[24:27], 0\n"
    "ds_read_b64_tr_b16 v[234:235], v20 offset:4608\n"
    "ds_read_b64_tr_b16 v[236:237], v21 offset:4096\n"
    "v_mfma_f32_16x16x32_bf16 v[172:175], v[208:211], v[28:31], 0\n"
    "s_and_b32 s62, s61, 31\n"
    "s_lshl_b32 s62, s62, 6\n"
    "s_add_u32 s62, s62, s5\n"
    "v_and_b32 v14, 0x23, v7\n"
    "v_and_b32 v12, 12, v7\n"
    "v_lshl_or_b32 v14, v12, 1, v14\n"
    "v_bfe_u32 v12, v7, 4, 1\n"
    "v_lshl_or_b32 v14, v12, 2, v14\n"
    "v_lshlrev_b32 v14, 4, v14\n"
    "s_cmp_lt_u32 s61, 32\n"
    "s_cselect_b32 s50, 0x3eb8aa3b, 1.0\n"
    "s_cselect_b32 s64, 10, 11\n"
    "s_cselect_b64 vcc, -1, 0\n"
    "s_cselect_b32 s61, 1, 3\n"
    "s_waitcnt lgkmcnt(13)\n"
    "v_mfma_f32_16x16x32_bf16 v[180:183], v[212:215], v[28:31], 0\n"
    "ds_read_b64_tr_b16 v[238:239], v21 offset:4608\n"
    "ds_read_b64_tr_b16 v[240:241], v22 offset:4096\n"
    "s_add_u32 m0, s56, 82944\n"
    "v_mfma_f32_16x16x32_bf16 v[176:179], v[212:215], v[24:27], 0\n"
    "buffer_load_dwordx4 v3, s[24:27], s57 offen lds\n"
    "s_mov_b32 s51, s50\n"
    "v_cndmask_b32 v248, v14, v1, vcc\n"
    "s_lshl_b32 s61, s61, 20\n"
    "s_lshl_b32 s62, s62, s64\n"
    "s_add_u32 s70, s61, s62\n"
    "s_lshl_b32 s71, 64, s64\n"
    "s_lshl_b32 s61, s53, 4\n"
    "s_add_u32 s73, s61, s66\n"
    "s_lshl_b32 s73, s73, 7\n"
    "s_add_u32 s73, s73, 655360\n"
    "s_lshl_b32 s61, s20, 7\n"
    "s_and_b32 s62, s66, 7\n"
    "s_mul_i32 s62, s62, 28672\n"
    "s_waitcnt lgkmcnt(13)\n"
    "v_mfma_f32_16x16x32_bf16 v[184:187], v[216:219], v[24:27], 0\n"
    "ds_read_b64_tr_b16 v[242:243], v22 offset:4608\n"
    "ds_read_b64_tr_b16 v[244:245], v23 offset:4096\n"
    "v_mfma_f32_16x16x32_bf16 v[188:191], v[216:219], v[28:31], 0\n"
    "s_add_u32 s74, s61, s62\n"
    "s_lshl_b32 s82, s66, 4\n"
    "s_add_u32 s82, s82, s53\n"
    "s_lshl_b32 s82, s82, 3\n"
    "s_add_u32 s82, s82, s52\n"
    "s_lshl_b32 s82, s82, 3\n"
    "s_add_u32 s82, s82, 8192\n"
    "s_lshl_b32 s6, s79, 2\n"
    "s_lshr_b32 s61, s68, 3\n"
    "s_add_u32 s6, s6, s61\n"
    "s_and_b32 s7, s68, 7\n"
    "s_and_b32 s16, s52, 1\n"
    "s_lshr_b32 s80, s52, 1\n"
    "s_waitcnt lgkmcnt(13)\n"
    "v_mfma_f32_16x16x32_bf16 v[196:199], v[220:223], v[28:31], 0\n"
    "ds_read_b64_tr_b16 v[246:247], v23 offset:4608\n"
    "s_add_u32 m0, s55, 98304\n"
    "v_mfma_f32_16x16x32_bf16 v[192:195], v[220:223], v[24:27], 0\n"
    "buffer_load_dwordx4 v4, s[28:31], s58 offen lds\n"
    "s_lshr_b32 s65, s6, 2\n"
    "s_lshr_b32 s62, s65, 2\n"
    "s_sub_u32 s61, s65, 12\n"
    "s_cmp_lt_u32 s65, 16\n"
    "s_cselect_b32 s61, s62, s61\n"
    "s_mul_i32 s95, s61, 11\n"
    "s_lshr_b32 s95, s95, 5\n"
    "s_add_u32 s61, s61, s95\n"
    "s_add_u32 s61, s61, 1\n"
    "s_lshl_b32 s62, s62, 2\n"
    "s_lshl_b32 s95, 1, s65\n"
    "s_and_b32 s95, s95, 0x7777\n"
    "s_cselect_b32 s62, s62, s61\n"
    "s_waitcnt lgkmcnt(10)\n"
    "v_mfma_f32_16x16x32_bf16 v[152:155], v[224:227], v[32:35], v[152:155]\n"
    "v_mfma_f32_16x16x32_bf16 v[156:159], v[224:227], v[36:39], v[156:159]\n"
    "s_lshl_b32 s62, s62, 4\n"
    "s_add_u32 s65, s65, 8\n"
    "s_lshr_b32 s63, s65, 2\n"
    "s_sub_u32 s61, s65, 12\n"
    "s_cmp_lt_u32 s65, 16\n"
    "s_cselect_b32 s61, s63, s61\n"
    "s_mul_i32 s95, s61, 11\n"
    "s_lshr_b32 s95, s95, 5\n"
    "s_add_u32 s61, s61, s95\n"
    "s_add_u32 s61, s61, 1\n"
    "s_lshl_b32 s63, s63, 2\n"
    "s_lshl_b32 s95, 1, s65\n"
    "s_and_b32 s95, s95, 0x7777\n"
    "s_cselect_b32 s63, s63, s61\n"
    "s_waitcnt lgkmcnt(8)\n"
    "v_mfma_f32_16x16x32_bf16 v[164:167], v[228:231], v[36:39], v[164:167]\n"
    "s_add_u32 m0, s55, 106496\n"
    "v_mfma_f32_16x16x32_bf16 v[160:163], v[228:231], v[32:35], v[160:163]\n"
    "buffer_load_dwordx4 v5, s[36:39], s59 offen lds\n"
    "s_lshl_b32 s63, s63, 4\n"
    "s_add_u32 s65, s65, 8\n"
    "s_lshr_b32 s64, s65, 2\n"
    "s_sub_u32 s61, s65, 12\n"
    "s_cmp_lt_u32 s65, 16\n"
    "s_cselect_b32 s61, s64, s61\n"
    "s_mul_i32 s95, s61, 11\n"
    "s_lshr_b32 s95, s95, 5\n"
    "s_add_u32 s61, s61, s95\n"
    "s_add_u32 s61, s61, 1\n"
    "s_lshl_b32 s64, s64, 2\n"
    "s_lshl_b32 s95, 1, s65\n"
    "s_and_b32 s95, s95, 0x7777\n"
    "s_cselect_b32 s64, s64, s61\n"
    "s_waitcnt lgkmcnt(0)\n"
    "s_waitcnt vmcnt(7)\n"
    "s_barrier\n"
    "v_mfma_f32_16x16x32_bf16 v[168:171], v[232:235], v[32:35], v[168:171]\n"
    "ds_read_b128 v[40:43], v16 offset:40960\n"
    "ds_read_b128 v[44:47], v16 offset:43008\n"
    "ds_read_b64_tr_b16 v[200:201], v18 offset:40960\n"
    "v_mfma_f32_16x16x32_bf16 v[172:175], v[232:235], v[36:39], v[172:175]\n"
    "ds_read_b64_tr_b16 v[202:203], v18 offset:41472\n"
    "ds_read_b64_tr_b16 v[204:205], v19 offset:40960\n"
    "ds_read_b64_tr_b16 v[206:207], v19 offset:41472\n"
    "s_lshl_b32 s64, s64, 4\n"
    "s_lshl_b32 s61, s7, 1\n"
    "s_add_u32 s62, s62, s61\n"
    "v_and_b32 v10, 15, v7\n"
    "v_and_b32 v11, 1, v7\n"
    "v_add_u32 v14, s63, v10\n"
    "v_add_u32 v12, s64, v10\n"
    "v_add_u32 v11, s62, v11\n"
    "v_cmp_le_u32 vcc, 16, v7\n"
    "s_lshl_b32 s61, s6, 6\n"
    "s_lshl_b32 s62, s7, 3\n"
    "v_cndmask_b32 v14, v14, v12, vcc\n"
    "v_cmp_le_u32 vcc, 32, v7\n"
    "v_mfma_f32_16x16x32_bf16 v[180:183], v[236:239], v[36:39], v[180:183]\n"
    "ds_read_b64_tr_b16 v[208:209], v20 offset:40960\n"
    "ds_read_b64_tr_b16 v[210:211], v20 offset:41472\n"
    "ds_read_b64_tr_b16 v[212:213], v21 offset:40960\n"
    "v_mfma_f32_16x16x32_bf16 v[176:179], v[236:239], v[32:35], v[176:179]\n"
    "ds_read_b64_tr_b16 v[214:215], v21 offset:41472\n"
    "ds_read_b64_tr_b16 v[216:217], v22 offset:40960\n"
    "s_add_u32 m0, s55, 114688\n"
    "ds_read_b64_tr_b16 v[218:219], v22 offset:41472\n"
    "buffer_load_dwordx4 v6, s[40:43], s59 offen lds\n"
    "v_mfma_f32_16x16x32_bf16 v[184:187], v[240:243], v[32:35], v[184:187]\n"
    "ds_read_b64_tr_b16 v[220:221], v23 offset:40960\n"
    "ds_read_b64_tr_b16 v[222:223], v23 offset:41472\n"
    "v_mfma_f32_16x16x32_bf16 v[188:191], v[240:243], v[36:39], v[188:191]\n"
    "v_mfma_f32_16x16x32_bf16 v[196:199], v[244:247], v[36:39], v[196:199]\n"
    "v_mfma_f32_16x16x32_bf16 v[192:195], v[244:247], v[32:35], v[192:195]\n"
    "s_waitcnt lgkmcnt(10)\n"
    "v_mfma_f32_16x16x32_bf16 v[152:155], v[200:203], v[40:43], v[152:155]\n"
    "ds_read_b128 v[48:51], v17 offset:40960\n"
    "ds_read_b128 v[52:55], v17 offset:43008\n"
    "v_mfma_f32_16x16x32_bf16 v[156:159], v[200:203], v[44:47], v[156:159]\n"
    "ds_read_b64_tr_b16 v[224:225], v18 offset:45056\n"
    "ds_read_b64_tr_b16 v[226:227], v18 offset:45568\n"
    "s_lshl_b32 s57, s54, 7\n"
    "s_mul_i32 s58, s54, 196608\n"
    "s_mul_i32 s59, s54, s60\n"
    "s_add_u32 s54, s54, 1\n"
    "s_and_b32 s54, s54, 7\n"
    "s_lshl_b32 s63, s16, 2\n"
    "s_add_u32 s62, s62, s63\n"
    "v_cndmask_b32 v14, v14, v11, vcc\n"
    "s_lshl_b32 s63, s80, 6\n"
    "s_lshr_b32 s64, s62, 2\n"
    "s_add_u32 s63, s63, s64\n"
    "v_and_b32 v11, 3, v7\n"
    "v_lshl_add_u32 v11, v11, 4, s63\n"
    "v_cmp_le_u32 vcc, 48, v7\n"
    "s_add_u32 s63, s62, s80\n"
    "s_lshl_b32 s63, s63, 7\n"
    "v_cndmask_b32 v14, v14, v11, vcc\n"
    "s_add_u32 s21, s63, 40960\n"
    "s_waitcnt lgkmcnt(12)\n"
    "v_mfma_f32_16x16x32_bf16 v[164:167], v[204:207], v[44:47], v[164:167]\n"
    "ds_read_b64_tr_b16 v[228:229], v19 offset:45056\n"
    "ds_read_b64_tr_b16 v[230:231], v19 offset:45568\n"
    "v_mfma_f32_16x16x32_bf16 v[160:163], v[204:207], v[40:43], v[160:163]\n"
    "s_add_u32 m0, s56, 122880\n"
    "ds_read_b64_tr_b16 v[232:233], v20 offset:45056\n"
    "buffer_load_dwordx4 v2, s[24:27], s57 offen lds\n"
    "v_lshlrev_b32 v14, 7, v14\n"
    "v_add_u32 v249, 655360, v14\n"
    "v_lshrrev_b32 v11, 5, v7\n"
    "v_lshlrev_b32 v11, 8, v11\n"
    "v_lshl_add_u32 v250, v10, 4, v11\n"
    "s_add_u32 s61, s61, s62\n"
    "s_lshl_b32 s61, s61, 10\n"
    "s_add_u32 s75, s61, 1048576\n"
    "s_lshl_b32 s61, s80, 10\n"
    "s_add_u32 s89, s75, s61\n"
    "s_add_u32 s62, s62, s80\n"
    "s_lshl_b32 s62, s62, 5\n"
    "s_add_u32 s62, s62, s6\n"
    "s_waitcnt lgkmcnt(13)\n"
    "v_mfma_f32_16x16x32_bf16 v[168:171], v[208:211], v[40:43], v[168:171]\n"
    "ds_read_b64_tr_b16 v[234:235], v20 offset:45568\n"
    "ds_read_b64_tr_b16 v[236:237], v21 offset:45056\n"
    "s_add_u32 m0, s56, 123904\n"
    "v_mfma_f32_16x16x32_bf16 v[172:175], v[208:211], v[44:47], v[172:175]\n"
    "buffer_load_dwordx4 v3, s[24:27], s57 offen lds\n"
    "s_lshl_b32 s90, s62, 10\n"
    "s_add_u32 s90, s90, 7340032\n"
    "s_lshl_b32 s91, s62, 7\n"
    "s_add_u32 s91, s91, 786432\n"
    "s_lshl_b32 s76, s6, 17\n"
    "s_add_u32 s76, s76, 3145728\n"
    "s_lshl_b32 s61, s80, 15\n"
    "s_add_u32 s77, s76, s61\n"
    "s_lshl_b32 s78, s7, 12\n"
    "s_lshl_b32 s17, s6, 1\n"
    "s_add_u32 s17, s17, s16\n"
    "s_lshr_b32 s61, s6, 1\n"
    "s_lshl_b32 s61, s61, 10\n"
    "s_waitcnt lgkmcnt(13)\n"
    "v_mfma_f32_16x16x32_bf16 v[180:183], v[212:215], v[44:47], v[180:183]\n"
    "ds_read_b64_tr_b16 v[238:239], v21 offset:45568\n"
    "ds_read_b64_tr_b16 v[240:241], v22 offset:45056\n"
    "s_add_u32 m0, s55, 139264\n"
    "v_mfma_f32_16x16x32_bf16 v[176:179], v[212:215], v[40:43], v[176:179]\n"
    "buffer_load_dwordx4 v4, s[28:31], s58 offen lds\n"
    "s_add_u32 s83, s61, 8192\n"
    "s_lshl_b32 s61, s17, 7\n"
    "s_add_u32 s84, s61, 40960\n"
    "s_lshl_b32 s61, s17, 12\n"
    "s_lshl_b32 s62, s80, 10\n"
    "s_add_u32 s61, s61, s62\n"
    "s_add_u32 s92, s61, 786432\n"
    "s_lshl_b32 s61, s17, 15\n"
    "s_lshl_b32 s62, s80, 13\n"
    "s_add_u32 s93, s61, s62\n"
    "s_add_u32 s93, s93, 7340032\n"
    "s_lshl_b32 s65, s7, 7\n"
    "s_add_u32 s61, s61, s65\n"
    "s_waitcnt lgkmcnt(13)\n"
    "v_mfma_f32_16x16x32_bf16 v[184:187], v[216:219], v[40:43], v[184:187]\n"
    "ds_read_b64_tr_b16 v[242:243], v22 offset:45568\n"
    "ds_read_b64_tr_b16 v[244:245], v23 offset:45056\n"
    "s_add_u32 m0, s55, 147456\n"
    "v_mfma_f32_16x16x32_bf16 v[188:191], v[216:219], v[44:47], v[188:191]\n"
    "buffer_load_dwordx4 v5, s[36:39], s59 offen lds\n"
    "s_add_u32 s94, s61, s62\n"
    "s_mul_i32 s63, s17, 112\n"
    "s_mul_i32 s64, s80, 14\n"
    "s_xor_b32 s61, s6, s7\n"
    "s_add_u32 s62, s61, 0\n"
    "s_and_b32 s62, s62, 1\n"
    "s_mul_i32 s62, s62, 7\n"
    "s_add_u32 s62, s62, s64\n"
    "s_lshl_b32 s96, s62, 15\n"
    "s_add_u32 s62, s62, s63\n"
    "s_lshl_b32 s62, s62, 10\n"
    "s_add_u32 s98, s62, 9437184\n"
    "s_add_u32 s62, s61, 1\n"
    "s_waitcnt lgkmcnt(13)\n"
    "v_mfma_f32_16x16x32_bf16 v[196:199], v[220:223], v[44:47], v[196:199]\n"
    "ds_read_b64_tr_b16 v[246:247], v23 offset:45568\n"
    "v_mfma_f32_16x16x32_bf16 v[192:195], v[220:223], v[40:43], v[192:195]\n"
    "s_and_b32 s62, s62, 1\n"
    "s_mul_i32 s62, s62, 7\n"
    "s_add_u32 s62, s62, s64\n"
    "s_lshl_b32 s97, s62, 15\n"
    "s_add_u32 s62, s62, s63\n"
    "s_lshl_b32 s62, s62, 10\n"
    "s_add_u32 s99, s62, 9437184\n"
    "s_lshl_b32 s61, s16, 11\n"
    "s_mul_i32 s95, s80, 24576\n"
    "s_add_u32 s95, s95, 65536\n"
    "s_add_u32 s87, s95, s61\n"
    "s_lshl_b32 s62, s80, 14\n"
    "s_add_u32 s88, s62, s61\n"
    "s_waitcnt lgkmcnt(10)\n"
    "v_mfma_f32_16x16x32_bf16 v[152:155], v[224:227], v[48:51], v[152:155]\n"
    "s_add_u32 m0, s55, 155648\n"
    "v_mfma_f32_16x16x32_bf16 v[156:159], v[224:227], v[52:55], v[156:159]\n"
    "buffer_load_dwordx4 v6, s[40:43], s59 offen lds\n"
    "s_sub_u32 s95, s95, s62\n"
    "s_and_b32 s61, s6, 3\n"
    "s_lshl_b32 s61, s61, 3\n"
    "s_add_u32 s61, s61, s52\n"
    "s_lshl_b32 s85, s61, 14\n"
    "s_add_u32 s85, s85, s65\n"
    "s_mul_i32 s62, s61, 57344\n"
    "s_add_u32 s86, s62, s65\n"
    "v_lshrrev_b32 v10, 3, v7\n"
    "s_lshl_b32 s62, s16, 4\n"
    "v_add_u32 v10, s62, v10\n"
    "v_lshlrev_b32 v10, 10, v10\n"
    "v_lshrrev_b32 v11, 4, v7\n"
    "s_waitcnt lgkmcnt(8)\n"
    "v_mfma_f32_16x16x32_bf16 v[164:167], v[228:231], v[52:55], v[164:167]\n"
    "v_mfma_f32_16x16x32_bf16 v[160:163], v[228:231], v[48:51], v[160:163]\n"
    "s_lshl_b32 s57, s54, 7\n"
    "s_mul_i32 s58, s54, 196608\n"
    "s_mul_i32 s59, s54, s60\n"
    "s_add_u32 s54, s54, 1\n"
    "s_and_b32 s54, s54, 7\n"
    "v_lshlrev_b32 v11, 1, v11\n"
    "v_and_b32 v12, 7, v7\n"
    "v_xor_b32 v11, v11, v12\n"
    "v_lshl_add_u32 v10, v11, 4, v10\n"
    "v_add_u32 v251, s65, v10\n"
    "v_bfe_u32 v10, v7, 2, 2\n"
    "v_lshrrev_b32 v11, 4, v7\n"
    "v_and_b32 v13, 3, v7\n"
    "v_lshlrev_b32 v13, 3, v13\n"
    "v_lshl_add_u32 v12, v11, 2, v10\n"
    "v_lshl_add_u32 v13, v12, 7, v13\n"
    "v_lshrrev_b32 v10, 1, v10\n"
    "v_and_b32 v12, 1, v11\n"
    "s_waitcnt lgkmcnt(0)\n"
    "s_waitcnt vmcnt(5)\n"
    "s_barrier\n"
    "v_add_u32 v16, 0x14000, v16\n"
    "v_add_u32 v17, 0x14000, v17\n"
    "v_add_u32 v18, 0x14000, v18\n"
    "v_add_u32 v19, 0x14000, v19\n"
    "v_add_u32 v20, 0x14000, v20\n"
    "v_add_u32 v21, 0x14000, v21\n"
    "v_add_u32 v22, 0x14000, v22\n"
    "v_add_u32 v23, 0x14000, v23\n"
    "v_mfma_f32_16x16x32_bf16 v[168:171], v[232:235], v[48:51], v[168:171]\n"
    "ds_read_b128 v[56:59], v16 offset:0\n"
    "ds_read_b128 v[60:63], v16 offset:2048\n"
    "ds_read_b64_tr_b16 v[200:201], v18 offset:0\n"
    "v_mfma_f32_16x16x32_bf16 v[172:175], v[232:235], v[52:55], v[172:175]\n"
    "ds_read_b64_tr_b16 v[202:203], v18 offset:512\n"
    "ds_read_b64_tr_b16 v[204:205], v19 offset:0\n"
    "s_add_u32 m0, s56, 0\n"
    "ds_read_b64_tr_b16 v[206:207], v19 offset:512\n"
    "buffer_load_dwordx4 v2, s[24:27], s57 offen lds\n"
    "v_lshl_or_b32 v10, v12, 1, v10\n"
    "v_lshl_or_b32 v13, v10, 5, v13\n"
    "s_and_b32 s63, s87, 0xfffff000\n"
    "v_add_u32 v253, s63, v13\n"
    "v_lshrrev_b32 v254, 5, v7\n"
    "v_lshlrev_b32 v255, 10, v254\n"
    "v_bfe_u32 v12, v7, 4, 1\n"
    "v_lshl_or_b32 v254, v12, 9, v255\n"
    "v_and_b32 v12, 15, v7\n"
    "v_lshl_or_b32 v254, v12, 4, v254\n"
    "v_mfma_f32_16x16x32_bf16 v[180:183], v[236:239], v[52:55], v[180:183]\n"
    "ds_read_b64_tr_b16 v[208:209], v20 offset:0\n"
    "ds_read_b64_tr_b16 v[210:211], v20 offset:512\n"
    "ds_read_b64_tr_b16 v[212:213], v21 offset:0\n"
    "v_mfma_f32_16x16x32_bf16 v[176:179], v[236:239], v[48:51], v[176:179]\n"
    "ds_read_b64_tr_b16 v[214:215], v21 offset:512\n"
    "ds_read_b64_tr_b16 v[216:217], v22 offset:0\n"
    "s_add_u32 m0, s56, 1024\n"
    "ds_read_b64_tr_b16 v[218:219], v22 offset:512\n"
    "buffer_load_dwordx4 v3, s[24:27], s57 offen lds\n"
    "v_mfma_f32_16x16x32_bf16 v[184:187], v[240:243], v[48:51], v[184:187]\n"
    "ds_read_b64_tr_b16 v[220:221], v23 offset:0\n"
    "ds_read_b64_tr_b16 v[222:223], v23 offset:512\n"
    "v_mfma_f32_16x16x32_bf16 v[188:191], v[240:243], v[52:55], v[188:191]\n"
    "v_mfma_f32_16x16x32_bf16 v[196:199], v[244:247], v[52:55], v[196:199]\n"
    "v_mfma_f32_16x16x32_bf16 v[192:195], v[244:247], v[48:51], v[192:195]\n"
    "s_waitcnt lgkmcnt(10)\n"
    "v_mfma_f32_16x16x32_bf16 v[152:155], v[200:203], v[56:59], v[152:155]\n"
    "ds_read_b128 v[64:67], v17 offset:0\n"
    "ds_read_b128 v[68:71], v17 offset:2048\n"
    "v_mfma_f32_16x16x32_bf16 v[156:159], v[200:203], v[60:63], v[156:159]\n"
    "ds_read_b64_tr_b16 v[224:225], v18 offset:4096\n"
    "s_add_u32 m0, s55, 16384\n"
    "ds_read_b64_tr_b16 v[226:227], v18 offset:4608\n"
    "buffer_load_dwordx4 v4, s[28:31], s58 offen lds\n"
    "s_waitcnt lgkmcnt(12)\n"
    "v_mfma_f32_16x16x32_bf16 v[164:167], v[204:207], v[60:63], v[164:167]\n"
    "ds_read_b64_tr_b16 v[228:229], v19 offset:4096\n"
    "ds_read_b64_tr_b16 v[230:231], v19 offset:4608\n"
    "v_mfma_f32_16x16x32_bf16 v[160:163], v[204:207], v[56:59], v[160:163]\n"
    "s_add_u32 m0, s55, 24576\n"
    "ds_read_b64_tr_b16 v[232:233], v20 offset:4096\n"
    "buffer_load_dwordx4 v5, s[36:39], s59 offen lds\n"
    "s_waitcnt lgkmcnt(13)\n"
    "v_mfma_f32_16x16x32_bf16 v[168:171], v[208:211], v[56:59], v[168:171]\n"
    "ds_read_b64_tr_b16 v[234:235], v20 offset:4608\n"
    "ds_read_b64_tr_b16 v[236:237], v21 offset:4096\n"
    "s_add_u32 m0, s55, 32768\n"
    "v_mfma_f32_16x16x32_bf16 v[172:175], v[208:211], v[60:63], v[172:175]\n"
    "buffer_load_dwordx4 v6, s[40:43], s59 offen lds\n"
    "s_waitcnt lgkmcnt(13)\n"
    "v_mfma_f32_16x16x32_bf16 v[180:183], v[212:215], v[60:63], v[180:183]\n"
    "ds_read_b64_tr_b16 v[238:239], v21 offset:4608\n"
    "ds_read_b64_tr_b16 v[240:241], v22 offset:4096\n"
    "v_mfma_f32_16x16x32_bf16 v[176:179], v[212:215], v[56:59], v[176:179]\n"
    "s_waitcnt lgkmcnt(13)\n"
    "v_mfma_f32_16x16x32_bf16 v[184:187], v[216:219], v[56:59], v[184:187]\n"
    "ds_read_b64_tr_b16 v[242:243], v22 offset:4608\n"
    "ds_read_b64_tr_b16 v[244:245], v23 offset:4096\n"
    "v_mfma_f32_16x16x32_bf16 v[188:191], v[216:219], v[60:63], v[188:191]\n"
    "s_lshl_b32 s57, s54, 7\n"
    "s_mul_i32 s58, s54, 196608\n"
    "s_mul_i32 s59, s54, s60\n"
    "s_add_u32 s54, s54, 1\n"
    "s_and_b32 s54, s54, 7\n"
    "s_waitcnt lgkmcnt(13)\n"
    "v_mfma_f32_16x16x32_bf16 v[196:199], v[220:223], v[60:63], v[196:199]\n"
    "ds_read_b64_tr_b16 v[246:247], v23 offset:4608\n"
    "s_add_u32 m0, s56, 40960\n"
    "v_mfma_f32_16x16x32_bf16 v[192:195], v[220:223], v[56:59], v[192:195]\n"
    "buffer_load_dwordx4 v2, s[24:27], s57 offen lds\n"
    "v_readfirstlane_b32 s22, v148\n"
    "v_readfirstlane_b32 s23, v149\n"
    "v_mov_b32 v14, 0\n"
    "s_nop 1\n"
    "s_add_u32 s22, s22, 1\n"
    "s_addc_u32 s23, s23, 0\n"
    "v_dot2_f32_bf16 v14, v136, v140, v14\n"
    "v_dot2_f32_bf16 v14, v137, v141, v14\n"
    "v_dot2_f32_bf16 v14, v138, v142, v14\n"
    "v_dot2_f32_bf16 v14, v139, v143, v14\n"
    "s_nop 2\n"
    "v_add_f32_dpp v14, v14, v14 row_shr:1 row_mask:0xf bank_mask:0xf bound_ctrl:0\n"
    "s_nop 1\n"
    "v_add_f32_dpp v14, v14, v14 row_shr:2 row_mask:0xf bank_mask:0xf bound_ctrl:0\n"
    "s_nop 1\n"
    "v_add_f32_dpp v14, v14, v14 row_shr:4 row_mask:0xf bank_mask:0xf bound_ctrl:0\n"
    "s_nop 1\n"
    "v_add_f32_dpp v14, v14, v14 row_shr:8 row_mask:0xf bank_mask:0xf bound_ctrl:0\n"
    "s_nop 1\n"
    "v_add_f32_dpp v14, v14, v14 row_bcast:15 row_mask:0xa bank_mask:0xf\n"
    "s_nop 1\n"
    "v_add_f32_dpp v14, v14, v14 row_bcast:31 row_mask:0xc bank_mask:0xf\n"
    "v_mul_f32 v14, 0xbfb8aa3b, v14\n"
    "v_exp_f32 v14, v14\n"
    "v_mov_b32 v13, 0\n"
    "v_add_f32 v14, 1.0, v14\n"
    "v_rcp_f32 v14, v14\n"
    "s_mov_b32 exec_lo, 0\n"
    "s_mov_b32 exec_hi, 0x80000000\n"
    "buffer_store_dword v14, v13, s[44:47], s69 offen sc1\n"
    "s_mov_b64 exec, -1\n"
    "s_waitcnt lgkmcnt(10)\n"
    "v_mfma_f32_16x16x32_bf16 v[152:155], v[224:227], v[64:67], v[152:155]\n"
    "s_add_u32 m0, s56, 41984\n"
    "v_mfma_f32_16x16x32_bf16 v[156:159], v[224:227], v[68:71], v[156:159]\n"
    "buffer_load_dwordx4 v3, s[24:27], s57 offen lds\n"
    "s_waitcnt lgkmcnt(8)\n"
    "v_mfma_f32_16x16x32_bf16 v[164:167], v[228:231], v[68:71], v[164:167]\n"
    "v_mfma_f32_16x16x32_bf16 v[160:163], v[228:231], v[64:67], v[160:163]\n"
    "s_waitcnt lgkmcnt(0)\n"
    "s_waitcnt vmcnt(8)\n"
    "s_barrier\n"
    "v_mfma_f32_16x16x32_bf16 v[168:171], v[232:235], v[64:67], v[168:171]\n"
    "ds_read_b128 v[72:75], v16 offset:40960\n"
    "ds_read_b128 v[76:79], v16 offset:43008\n"
    "ds_read_b64_tr_b16 v[200:201], v18 offset:40960\n"
    "v_mfma_f32_16x16x32_bf16 v[172:175], v[232:235], v[68:71], v[172:175]\n"
    "ds_read_b64_tr_b16 v[202:203], v18 offset:41472\n"
    "ds_read_b64_tr_b16 v[204:205], v19 offset:40960\n"
    "s_add_u32 m0, s55, 57344\n"
    "ds_read_b64_tr_b16 v[206:207], v19 offset:41472\n"
    "buffer_load_dwordx4 v4, s[28:31], s58 offen lds\n"
    "v_mfma_f32_16x16x32_bf16 v[180:183], v[236:239], v[68:71], v[180:183]\n"
    "ds_read_b64_tr_b16 v[208:209], v20 offset:40960\n"
    "ds_read_b64_tr_b16 v[210:211], v20 offset:41472\n"
    "ds_read_b64_tr_b16 v[212:213], v21 offset:40960\n"
    "v_mfma_f32_16x16x32_bf16 v[176:179], v[236:239], v[64:67], v[176:179]\n"
    "ds_read_b64_tr_b16 v[214:215], v21 offset:41472\n"
    "ds_read_b64_tr_b16 v[216:217], v22 offset:40960\n"
    "s_add_u32 m0, s55, 65536\n"
    "ds_read_b64_tr_b16 v[218:219], v22 offset:41472\n"
    "buffer_load_dwordx4 v5, s[36:39], s59 offen lds\n"
    "v_mfma_f32_16x16x32_bf16 v[184:187], v[240:243], v[64:67], v[184:187]\n"
    "ds_read_b64_tr_b16 v[220:221], v23 offset:40960\n"
    "ds_read_b64_tr_b16 v[222:223], v23 offset:41472\n"
    "v_mfma_f32_16x16x32_bf16 v[188:191], v[240:243], v[68:71], v[188:191]\n"
    "v_mfma_f32_16x16x32_bf16 v[196:199], v[244:247], v[68:71], v[196:199]\n"
    "v_mfma_f32_16x16x32_bf16 v[192:195], v[244:247], v[64:67], v[192:195]\n"
    "s_waitcnt lgkmcnt(10)\n"
    "v_mfma_f32_16x16x32_bf16 v[152:155], v[200:203], v[72:75], v[152:155]\n"
    "ds_read_b128 v[80:83], v17 offset:40960\n"
    "ds_read_b128 v[84:87], v17 offset:43008\n"
    "v_mfma_f32_16x16x32_bf16 v[156:159], v[200:203], v[76:79], v[156:159]\n"
    "ds_read_b64_tr_b16 v[224:225], v18 offset:45056\n"
    "s_add_u32 m0, s55, 73728\n"
    "ds_read_b64_tr_b16 v[226:227], v18 offset:45568\n"
    "buffer_load_dwordx4 v6, s[40:43], s59 offen lds\n"
    "s_waitcnt lgkmcnt(12)\n"
    "v_mfma_f32_16x16x32_bf16 v[164:167], v[204:207], v[76:79], v[164:167]\n"
    "ds_read_b64_tr_b16 v[228:229], v19 offset:45056\n"
    "ds_read_b64_tr_b16 v[230:231], v19 offset:45568\n"
    "v_mfma_f32_16x16x32_bf16 v[160:163], v[204:207], v[72:75], v[160:163]\n"
    "ds_read_b64_tr_b16 v[232:233], v20 offset:45056\n"
    "s_lshl_b32 s57, s54, 7\n"
    "s_mul_i32 s58, s54, 196608\n"
    "s_mul_i32 s59, s54, s60\n"
    "s_add_u32 s54, s54, 1\n"
    "s_and_b32 s54, s54, 7\n"
    "s_waitcnt lgkmcnt(13)\n"
    "v_mfma_f32_16x16x32_bf16 v[168:171], v[208:211], v[72:75], v[168:171]\n"
    "ds_read_b64_tr_b16 v[234:235], v20 offset:45568\n"
    "ds_read_b64_tr_b16 v[236:237], v21 offset:45056\n"
    "s_add_u32 m0, s56, 81920\n"
    "v_mfma_f32_16x16x32_bf16 v[172:175], v[208:211], v[76:79], v[172:175]\n"
    "buffer_load_dwordx4 v2, s[24:27], s57 offen lds\n"
    "s_waitcnt lgkmcnt(13)\n"
    "v_mfma_f32_16x16x32_bf16 v[180:183], v[212:215], v[76:79], v[180:183]\n"
    "ds_read_b64_tr_b16 v[238:239], v21 offset:45568\n"
    "ds_read_b64_tr_b16 v[240:241], v22 offset:45056\n"
    "v_mfma_f32_16x16x32_bf16 v[176:179], v[212:215], v[72:75], v[176:179]\n"
    "s_waitcnt lgkmcnt(13)\n"
    "v_mfma_f32_16x16x32_bf16 v[184:187], v[216:219], v[72:75], v[184:187]\n"
    "ds_read_b64_tr_b16 v[242:243], v22 offset:45568\n"
    "ds_read_b64_tr_b16 v[244:245], v23 offset:45056\n"
    "s_add_u32 m0, s56, 82944\n"
    "v_mfma_f32_16x16x32_bf16 v[188:191], v[216:219], v[76:79], v[188:191]\n"
    "buffer_load_dwordx4 v3, s[24:27], s57 offen lds\n"
    "s_waitcnt lgkmcnt(13)\n"
    "v_mfma_f32_16x16x32_bf16 v[196:199], v[220:223], v[76:79], v[196:199]\n"
    "ds_read_b64_tr_b16 v[246:247], v23 offset:45568\n"
    "s_add_u32 m0, s55, 98304\n"
    "v_mfma_f32_16x16x32_bf16 v[192:195], v[220:223], v[72:75], v[192:195]\n"
    "buffer_load_dwordx4 v4, s[28:31], s58 offen lds\n"
    "s_waitcnt lgkmcnt(10)\n"
    "v_mfma_f32_16x16x32_bf16 v[152:155], v[224:227], v[80:83], v[152:155]\n"
    "s_add_u32 m0, s55, 106496\n"
    "v_mfma_f32_16x16x32_bf16 v[156:159], v[224:227], v[84:87], v[156:159]\n"
    "buffer_load_dwordx4 v5, s[36:39], s59 offen lds\n"
    "s_waitcnt lgkmcnt(8)\n"
    "v_mfma_f32_16x16x32_bf16 v[164:167], v[228:231], v[84:87], v[164:167]\n"
    "v_mfma_f32_16x16x32_bf16 v[160:163], v[228:231], v[80:83], v[160:163]\n"
    "s_waitcnt lgkmcnt(0)\n"
    "s_waitcnt vmcnt(10)\n"
    "s_barrier\n"
    "v_add_u32 v16, 0xfffec000, v16\n"
    "v_add_u32 v17, 0xfffec000, v17\n"
    "v_add_u32 v18, 0xfffec000, v18\n"
    "v_add_u32 v19, 0xfffec000, v19\n"
    "v_add_u32 v20, 0xfffec000, v20\n"
    "v_add_u32 v21, 0xfffec000, v21\n"
    "v_add_u32 v22, 0xfffec000, v22\n"
    "v_add_u32 v23, 0xfffec000, v23\n"
    "v_mfma_f32_16x16x32_bf16 v[168:171], v[232:235], v[80:83], v[168:171]\n"
    "ds_read_b128 v[88:91], v16 offset:0\n"
    "ds_read_b128 v[92:95], v16 offset:2048\n"
    "ds_read_b64_tr_b16 v[200:201], v18 offset:0\n"
    "v_mfma_f32_16x16x32_bf16 v[172:175], v[232:235], v[84:87], v[172:175]\n"
    "ds_read_b64_tr_b16 v[202:203], v18 offset:512\n"
    "ds_read_b64_tr_b16 v[204:205], v19 offset:0\n"
    "s_add_u32 m0, s55, 114688\n"
    "ds_read_b64_tr_b16 v[206:207], v19 offset:512\n"
    "buffer_load_dwordx4 v6, s[40:43], s59 offen lds\n"
    "v_mfma_f32_16x16x32_bf16 v[180:183], v[236:239], v[84:87], v[180:183]\n"
    "ds_read_b64_tr_b16 v[208:209], v20 offset:0\n"
    "ds_read_b64_tr_b16 v[210:211], v20 offset:512\n"
    "ds_read_b64_tr_b16 v[212:213], v21 offset:0\n"
    "v_mfma_f32_16x16x32_bf16 v[176:179], v[236:239], v[80:83], v[176:179]\n"
    "ds_read_b64_tr_b16 v[214:215], v21 offset:512\n"
    "ds_read_b64_tr_b16 v[216:217], v22 offset:0\n"
    "ds_read_b64_tr_b16 v[218:219], v22 offset:512\n"
    "s_bitcmp1_b32 s52, 0\n"
    "s_cselect_b64 s[48:49], s[12:13], s[10:11]\n"
    "s_lshr_b32 s61, s52, 1\n"
    "s_add_u32 s61, s61, s54\n"
    "s_add_u32 s61, s61, 1\n"
    "s_and_b32 s61, s61, 7\n"
    "s_mul_i32 s61, s61, 229376\n"
    "s_add_u32 s61, s61, s74\n"
    "v_lshrrev_b32 v10, 3, v7\n"
    "v_mul_u32_u24 v10, 3584, v10\n"
    "v_and_b32 v11, 0x70, v1\n"
    "v_add3_u32 v10, v10, v11, s61\n"
    "global_load_dwordx4 v[144:147], v10, s[48:49]\n"
    "v_mfma_f32_16x16x32_bf16 v[184:187], v[240:243], v[80:83], v[184:187]\n"
    "ds_read_b64_tr_b16 v[220:221], v23 offset:0\n"
    "ds_read_b64_tr_b16 v[222:223], v23 offset:512\n"
    "v_mfma_f32_16x16x32_bf16 v[188:191], v[240:243], v[84:87], v[188:191]\n"
    "v_mfma_f32_16x16x32_bf16 v[196:199], v[244:247], v[84:87], v[196:199]\n"
    "v_mfma_f32_16x16x32_bf16 v[192:195], v[244:247], v[80:83], v[192:195]\n"
    "s_waitcnt lgkmcnt(10)\n"
    "v_mfma_f32_16x16x32_bf16 v[152:155], v[200:203], v[88:91], v[152:155]\n"
    "ds_read_b128 v[96:99], v17 offset:0\n"
    "ds_read_b128 v[100:103], v17 offset:2048\n"
    "v_mfma_f32_16x16x32_bf16 v[156:159], v[200:203], v[92:95], v[156:159]\n"
    "ds_read_b64_tr_b16 v[224:225], v18 offset:4096\n"
    "ds_read_b64_tr_b16 v[226:227], v18 offset:4608\n"
    "s_lshl_b32 s57, s54, 7\n"
    "s_mul_i32 s58, s54, 196608\n"
    "s_mul_i32 s59, s54, s60\n"
    "s_add_u32 s54, s54, 1\n"
    "s_and_b32 s54, s54, 7\n"
    "s_waitcnt lgkmcnt(12)\n"
    "v_mfma_f32_16x16x32_bf16 v[164:167], v[204:207], v[92:95], v[164:167]\n"
    "ds_read_b64_tr_b16 v[228:229], v19 offset:4096\n"
    "ds_read_b64_tr_b16 v[230:231], v19 offset:4608\n"
    "v_mfma_f32_16x16x32_bf16 v[160:163], v[204:207], v[88:91], v[160:163]\n"
    "s_add_u32 m0, s56, 122880\n"
    "ds_read_b64_tr_b16 v[232:233], v20 offset:4096\n"
    "buffer_load_dwordx4 v2, s[24:27], s57 offen lds\n"
    "s_waitcnt lgkmcnt(13)\n"
    "v_mfma_f32_16x16x32_bf16 v[168:171], v[208:211], v[88:91], v[168:171]\n"
    "ds_read_b64_tr_b16 v[234:235], v20 offset:4608\n"
    "ds_read_b64_tr_b16 v[236:237], v21 offset:4096\n"
    "v_mfma_f32_16x16x32_bf16 v[172:175], v[208:211], v[92:95], v[172:175]\n"
    "s_waitcnt lgkmcnt(13)\n"
    "v_mfma_f32_16x16x32_bf16 v[180:183], v[212:215], v[92:95], v[180:183]\n"
    "ds_read_b64_tr_b16 v[238:239], v21 offset:4608\n"
    "ds_read_b64_tr_b16 v[240:241], v22 offset:4096\n"
    "s_add_u32 m0, s56, 123904\n"
    "v_mfma_f32_16x16x32_bf16 v[176:179], v[212:215], v[88:91], v[176:179]\n"
    "buffer_load_dwordx4 v3, s[24:27], s57 offen lds\n"
    "s_waitcnt lgkmcnt(13)\n"
    "v_mfma_f32_16x16x32_bf16 v[184:187], v[216:219], v[88:91], v[184:187]\n"
    "ds_read_b64_tr_b16 v[242:243], v22 offset:4608\n"
    "ds_read_b64_tr_b16 v[244:245], v23 offset:4096\n"
    "v_mfma_f32_16x16x32_bf16 v[188:191], v[216:219], v[92:95], v[188:191]\n"
    "s_waitcnt lgkmcnt(13)\n"
    "v_mfma_f32_16x16x32_bf16 v[196:199], v[220:223], v[92:95], v[196:199]\n"
    "ds_read_b64_tr_b16 v[246:247], v23 offset:4608\n"
    "s_add_u32 m0, s55, 139264\n"
    "v_mfma_f32_16x16x32_bf16 v[192:195], v[220:223], v[88:91], v[192:195]\n"
    "buffer_load_dwordx4 v4, s[28:31], s58 offen lds\n"
    "s_waitcnt lgkmcnt(10)\n"
    "v_mfma_f32_16x16x32_bf16 v[152:155], v[224:227], v[96:99], v[152:155]\n"
    "v_mfma_f32_16x16x32_bf16 v[156:159], v[224:227], v[100:103], v[156:159]\n"
    "s_waitcnt lgkmcnt(8)\n"
    "v_mfma_f32_16x16x32_bf16 v[164:167], v[228:231], v[100:103], v[164:167]\n"
    "s_add_u32 m0, s55, 147456\n"
    "v_mfma_f32_16x16x32_bf16 v[160:163], v[228:231], v[96:99], v[160:163]\n"
    "buffer_load_dwordx4 v5, s[36:39], s59 offen lds\n"
    "s_waitcnt lgkmcnt(0)\n"
    "s_waitcnt vmcnt(10)\n"
    "s_barrier\n"
    "v_mfma_f32_16x16x32_bf16 v[168:171], v[232:235], v[96:99], v[168:171]\n"
    "ds_read_b128 v[104:107], v16 offset:40960\n"
    "ds_read_b128 v[108:111], v16 offset:43008\n"
    "ds_read_b64_tr_b16 v[200:201], v18 offset:40960\n"
    "v_mfma_f32_16x16x32_bf16 v[172:175], v[232:235], v[100:103], v[172:175]\n"
    "ds_read_b64_tr_b16 v[202:203], v18 offset:41472\n"
    "ds_read_b64_tr_b16 v[204:205], v19 offset:40960\n"
    "ds_read_b64_tr_b16 v[206:207], v19 offset:41472\n"
    "v_mfma_f32_16x16x32_bf16 v[180:183], v[236:239], v[100:103], v[180:183]\n"
    "ds_read_b64_tr_b16 v[208:209], v20 offset:40960\n"
    "ds_read_b64_tr_b16 v[210:211], v20 offset:41472\n"
    "ds_read_b64_tr_b16 v[212:213], v21 offset:40960\n"
    "v_mfma_f32_16x16x32_bf16 v[176:179], v[236:239], v[96:99], v[176:179]\n"
    "ds_read_b64_tr_b16 v[214:215], v21 offset:41472\n"
    "ds_read_b64_tr_b16 v[216:217], v22 offset:40960\n"
    "s_add_u32 m0, s55, 155648\n"
    "ds_read_b64_tr_b16 v[218:219], v22 offset:41472\n"
    "buffer_load_dwordx4 v6, s[40:43], s59 offen lds\n"
    "v_mfma_f32_16x16x32_bf16 v[184:187], v[240:243], v[96:99], v[184:187]\n"
    "ds_read_b64_tr_b16 v[220:221], v23 offset:40960\n"
    "ds_read_b64_tr_b16 v[222:223], v23 offset:41472\n"
    "v_mfma_f32_16x16x32_bf16 v[188:191], v[240:243], v[100:103], v[188:191]\n"
    "v_mfma_f32_16x16x32_bf16 v[196:199], v[244:247], v[100:103], v[196:199]\n"
    "v_mfma_f32_16x16x32_bf16 v[192:195], v[244:247], v[96:99], v[192:195]\n"
    "s_waitcnt lgkmcnt(10)\n"
    "v_mfma_f32_16x16x32_bf16 v[152:155], v[200:203], v[104:107], v[152:155]\n"
    "ds_read_b128 v[112:115], v17 offset:40960\n"
    "ds_read_b128 v[116:119], v17 offset:43008\n"
    "v_mfma_f32_16x16x32_bf16 v[156:159], v[200:203], v[108:111], v[156:159]\n"
    "ds_read_b64_tr_b16 v[224:225], v18 offset:45056\n"
    "ds_read_b64_tr_b16 v[226:227], v18 offset:45568\n"
    "s_mul_i32 s61, %2, 7\n"
    "s_add_u32 s61, s61, s52\n"
    "s_lshl_b32 s62, %2, 1\n"
    "s_cmp_eq_u32 s52, 7\n"
    "s_cselect_b64 s[48:49], s[8:9], s[14:15]\n"
    "s_cselect_b32 s61, s62, s61\n"
    "s_cselect_b32 s62, 15, 7\n"
    "s_lshl_b32 s61, s61, 10\n"
    "v_and_b32 v10, s62, v7\n"
    "v_lshl_add_u32 v10, v10, 7, s61\n"
    "global_load_dword v9, v10, s[48:49]\n"
    "s_waitcnt lgkmcnt(12)\n"
    "v_mfma_f32_16x16x32_bf16 v[164:167], v[204:207], v[108:111], v[164:167]\n"
    "ds_read_b64_tr_b16 v[228:229], v19 offset:45056\n"
    "ds_read_b64_tr_b16 v[230:231], v19 offset:45568\n"
    "v_mfma_f32_16x16x32_bf16 v[160:163], v[204:207], v[104:107], v[160:163]\n"
    "ds_read_b64_tr_b16 v[232:233], v20 offset:45056\n"
    "s_mov_b64 s[36:37], s[10:11]\n"
    "s_mov_b32 s38, 1835008\n"
    "s_mov_b32 s39, 0x20000\n"
    "s_mov_b64 s[40:41], s[12:13]\n"
    "s_mov_b32 s42, 1835008\n"
    "s_mov_b32 s43, 0x20000\n"
    "v_lshrrev_b32 v10, 3, v7\n"
    "s_lshl_b32 s61, s52, 3\n"
    "v_add_u32 v10, s61, v10\n"
    "v_mul_u32_u24 v10, 3584, v10\n"
    "v_and_b32 v5, 0x70, v4\n"
    "s_lshl_b32 s61, s20, 7\n"
    "v_add3_u32 v5, v5, v10, s61\n"
    "s_waitcnt lgkmcnt(13)\n"
    "v_mfma_f32_16x16x32_bf16 v[168:171], v[208:211], v[104:107], v[168:171]\n"
    "ds_read_b64_tr_b16 v[234:235], v20 offset:45568\n"
    "ds_read_b64_tr_b16 v[236:237], v21 offset:45056\n"
    "v_mfma_f32_16x16x32_bf16 v[172:175], v[208:211], v[108:111], v[172:175]\n"
    "s_waitcnt lgkmcnt(13)\n"
    "v_mfma_f32_16x16x32_bf16 v[180:183], v[212:215], v[108:111], v[180:183]\n"
    "ds_read_b64_tr_b16 v[238:239], v21 offset:45568\n"
    "ds_read_b64_tr_b16 v[240:241], v22 offset:45056\n"
    "s_add_u32 m0, s55, 0\n"
    "v_mfma_f32_16x16x32_bf16 v[176:179], v[212:215], v[104:107], v[176:179]\n"
    "s_mul_i32 s59, s54, 229376\n"
    "s_add_u32 s54, s54, 1\n"
    "s_and_b32 s54, s54, 7\n"
    "buffer_load_dwordx4 v5, s[36:39], s59 offen lds\n"
    "s_waitcnt lgkmcnt(13)\n"
    "v_mfma_f32_16x16x32_bf16 v[184:187], v[216:219], v[104:107], v[184:187]\n"
    "ds_read_b64_tr_b16 v[242:243], v22 offset:45568\n"
    "ds_read_b64_tr_b16 v[244:245], v23 offset:45056\n"
    "v_mfma_f32_16x16x32_bf16 v[188:191], v[216:219], v[108:111], v[188:191]\n"
    "s_waitcnt lgkmcnt(13)\n"
    "v_mfma_f32_16x16x32_bf16 v[196:199], v[220:223], v[108:111], v[196:199]\n"
    "ds_read_b64_tr_b16 v[246:247], v23 offset:45568\n"
    "s_add_u32 m0, s55, 8192\n"
    "v_mfma_f32_16x16x32_bf16 v[192:195], v[220:223], v[104:107], v[192:195]\n"
    "buffer_load_dwordx4 v5, s[40:43], s59 offen lds\n"
    "s_waitcnt lgkmcnt(10)\n"
    "v_mfma_f32_16x16x32_bf16 v[152:155], v[224:227], v[112:115], v[152:155]\n"
    "v_mfma_f32_16x16x32_bf16 v[156:159], v[224:227], v[116:119], v[156:159]\n"
    "s_waitcnt lgkmcnt(8)\n"
    "v_mfma_f32_16x16x32_bf16 v[164:167], v[228:231], v[116:119], v[164:167]\n"
    "s_add_u32 m0, s55, 16384\n"
    "v_mfma_f32_16x16x32_bf16 v[160:163], v[228:231], v[112:115], v[160:163]\n"
    "s_mul_i32 s59, s54, 229376\n"
    "s_add_u32 s54, s54, 1\n"
    "s_and_b32 s54, s54, 7\n"
    "buffer_load_dwordx4 v5, s[36:39], s59 offen lds\n"
    "s_waitcnt lgkmcnt(0)\n"
    "s_waitcnt vmcnt(10)\n"
    "s_barrier\n"
    "v_add_u32 v16, 0x14000, v16\n"
    "v_add_u32 v17, 0x14000, v17\n"
    "v_add_u32 v18, 0x14000, v18\n"
    "v_add_u32 v19, 0x14000, v19\n"
    "v_add_u32 v20, 0x14000, v20\n"
    "v_add_u32 v21, 0x14000, v21\n"
    "v_add_u32 v22, 0x14000, v22\n"
    "v_add_u32 v23, 0x14000, v23\n"
    "v_mfma_f32_16x16x32_bf16 v[168:171], v[232:235], v[112:115], v[168:171]\n"
    "ds_read_b128 v[120:123], v16 offset:0\n"
    "ds_read_b128 v[124:127], v16 offset:2048\n"
    "ds_read_b64_tr_b16 v[200:201], v18 offset:0\n"
    "v_mfma_f32_16x16x32_bf16 v[172:175], v[232:235], v[116:119], v[172:175]\n"
    "ds_read_b64_tr_b16 v[202:203], v18 offset:512\n"
    "ds_read_b64_tr_b16 v[204:205], v19 offset:0\n"
    "ds_read_b64_tr_b16 v[206:207], v19 offset:512\n"
    "v_mfma_f32_16x16x32_bf16 v[180:183], v[236:239], v[116:119], v[180:183]\n"
    "ds_read_b64_tr_b16 v[208:209], v20 offset:0\n"
    "ds_read_b64_tr_b16 v[210:211], v20 offset:512\n"
    "ds_read_b64_tr_b16 v[212:213], v21 offset:0\n"
    "v_mfma_f32_16x16x32_bf16 v[176:179], v[236:239], v[112:115], v[176:179]\n"
    "ds_read_b64_tr_b16 v[214:215], v21 offset:512\n"
    "ds_read_b64_tr_b16 v[216:217], v22 offset:0\n"
    "s_add_u32 m0, s55, 24576\n"
    "ds_read_b64_tr_b16 v[218:219], v22 offset:512\n"
    "buffer_load_dwordx4 v5, s[40:43], s59 offen lds\n"
    "v_mfma_f32_16x16x32_bf16 v[184:187], v[240:243], v[112:115], v[184:187]\n"
    "ds_read_b64_tr_b16 v[220:221], v23 offset:0\n"
    "ds_read_b64_tr_b16 v[222:223], v23 offset:512\n"
    "v_mfma_f32_16x16x32_bf16 v[188:191], v[240:243], v[116:119], v[188:191]\n"
    "v_mfma_f32_16x16x32_bf16 v[196:199], v[244:247], v[116:119], v[196:199]\n"
    "v_mfma_f32_16x16x32_bf16 v[192:195], v[244:247], v[112:115], v[192:195]\n"
    "s_waitcnt lgkmcnt(10)\n"
    "v_mfma_f32_16x16x32_bf16 v[152:155], v[200:203], v[120:123], v[152:155]\n"
    "ds_read_b128 v[128:131], v17 offset:0\n"
    "ds_read_b128 v[132:135], v17 offset:2048\n"
    "v_mfma_f32_16x16x32_bf16 v[156:159], v[200:203], v[124:127], v[156:159]\n"
    "ds_read_b64_tr_b16 v[224:225], v18 offset:4096\n"
    "s_add_u32 m0, s55, 40960\n"
    "ds_read_b64_tr_b16 v[226:227], v18 offset:4608\n"
    "s_mul_i32 s59, s54, 229376\n"
    "s_add_u32 s54, s54, 1\n"
    "s_and_b32 s54, s54, 7\n"
    "buffer_load_dwordx4 v5, s[36:39], s59 offen lds\n"
    "s_waitcnt lgkmcnt(12)\n"
    "v_mfma_f32_16x16x32_bf16 v[164:167], v[204:207], v[124:127], v[164:167]\n"
    "ds_read_b64_tr_b16 v[228:229], v19 offset:4096\n"
    "ds_read_b64_tr_b16 v[230:231], v19 offset:4608\n"
    "v_mfma_f32_16x16x32_bf16 v[160:163], v[204:207], v[120:123], v[160:163]\n"
    "ds_read_b64_tr_b16 v[232:233], v20 offset:4096\n"
    "s_waitcnt lgkmcnt(13)\n"
    "v_mfma_f32_16x16x32_bf16 v[168:171], v[208:211], v[120:123], v[168:171]\n"
    "ds_read_b64_tr_b16 v[234:235], v20 offset:4608\n"
    "ds_read_b64_tr_b16 v[236:237], v21 offset:4096\n"
    "s_add_u32 m0, s55, 49152\n"
    "v_mfma_f32_16x16x32_bf16 v[172:175], v[208:211], v[124:127], v[172:175]\n"
    "buffer_load_dwordx4 v5, s[40:43], s59 offen lds\n"
    "s_waitcnt lgkmcnt(13)\n"
    "v_mfma_f32_16x16x32_bf16 v[180:183], v[212:215], v[124:127], v[180:183]\n"
    "ds_read_b64_tr_b16 v[238:239], v21 offset:4608\n"
    "ds_read_b64_tr_b16 v[240:241], v22 offset:4096\n"
    "v_mfma_f32_16x16x32_bf16 v[176:179], v[212:215], v[120:123], v[176:179]\n"
    "s_waitcnt lgkmcnt(13)\n"
    "v_mfma_f32_16x16x32_bf16 v[184:187], v[216:219], v[120:123], v[184:187]\n"
    "ds_read_b64_tr_b16 v[242:243], v22 offset:4608\n"
    "ds_read_b64_tr_b16 v[244:245], v23 offset:4096\n"
    "v_mfma_f32_16x16x32_bf16 v[188:191], v[216:219], v[124:127], v[188:191]\n"
    "s_waitcnt lgkmcnt(13)\n"
    "v_mfma_f32_16x16x32_bf16 v[196:199], v[220:223], v[124:127], v[196:199]\n"
    "ds_read_b64_tr_b16 v[246:247], v23 offset:4608\n"
    "s_add_u32 m0, s55, 57344\n"
    "v_mfma_f32_16x16x32_bf16 v[192:195], v[220:223], v[120:123], v[192:195]\n"
    "s_mul_i32 s59, s54, 229376\n"
    "s_add_u32 s54, s54, 1\n"
    "s_and_b32 s54, s54, 7\n"
    "buffer_load_dwordx4 v5, s[36:39], s59 offen lds\n"
    "s_waitcnt lgkmcnt(10)\n"
    "v_mfma_f32_16x16x32_bf16 v[152:155], v[224:227], v[128:131], v[152:155]\n"
    "v_mfma_f32_16x16x32_bf16 v[156:159], v[224:227], v[132:135], v[156:159]\n"
    "s_waitcnt lgkmcnt(8)\n"
    "v_mfma_f32_16x16x32_bf16 v[164:167], v[228:231], v[132:135], v[164:167]\n"
    "v_mfma_f32_16x16x32_bf16 v[160:163], v[228:231], v[128:131], v[160:163]\n"
    "s_waitcnt lgkmcnt(0)\n"
    "s_waitcnt vmcnt(8)\n"
    "s_barrier\n"
    "v_mfma_f32_16x16x32_bf16 v[168:171], v[232:235], v[128:131], v[168:171]\n"
    "ds_read_b128 v[136:139], v16 offset:40960\n"
    "ds_read_b128 v[140:143], v16 offset:43008\n"
    "ds_read_b64_tr_b16 v[200:201], v18 offset:40960\n"
    "v_mfma_f32_16x16x32_bf16 v[172:175], v[232:235], v[132:135], v[172:175]\n"
    "ds_read_b64_tr_b16 v[202:203], v18 offset:41472\n"
    "ds_read_b64_tr_b16 v[204:205], v19 offset:40960\n"
    "s_add_u32 m0, s55, 65536\n"
    "ds_read_b64_tr_b16 v[206:207], v19 offset:41472\n"
    "buffer_load_dwordx4 v5, s[40:43], s59 offen lds\n"
    "v_mfma_f32_16x16x32_bf16 v[180:183], v[236:239], v[132:135], v[180:183]\n"
    "ds_read_b64_tr_b16 v[208:209], v20 offset:40960\n"
    "ds_read_b64_tr_b16 v[210:211], v20 offset:41472\n"
    "ds_read_b64_tr_b16 v[212:213], v21 offset:40960\n"
    "v_mfma_f32_16x16x32_bf16 v[176:179], v[236:239], v[128:131], v[176:179]\n"
    "ds_read_b64_tr_b16 v[214:215], v21 offset:41472\n"
    "ds_read_b64_tr_b16 v[216:217], v22 offset:40960\n"
    "ds_read_b64_tr_b16 v[218:219], v22 offset:41472\n"
    "v_mfma_f32_16x16x32_bf16 v[184:187], v[240:243], v[128:131], v[184:187]\n"
    "ds_read_b64_tr_b16 v[220:221], v23 offset:40960\n"
    "ds_read_b64_tr_b16 v[222:223], v23 offset:41472\n"
    "v_mfma_f32_16x16x32_bf16 v[188:191], v[240:243], v[132:135], v[188:191]\n"
    "v_mfma_f32_16x16x32_bf16 v[196:199], v[244:247], v[132:135], v[196:199]\n"
    "v_mfma_f32_16x16x32_bf16 v[192:195], v[244:247], v[128:131], v[192:195]\n"
    "s_waitcnt lgkmcnt(10)\n"
    "v_mfma_f32_16x16x32_bf16 v[152:155], v[200:203], v[136:139], v[152:155]\n"
    "ds_read_b128 v[144:147], v17 offset:40960\n"
    "ds_read_b128 v[148:151], v17 offset:43008\n"
    "v_mfma_f32_16x16x32_bf16 v[156:159], v[200:203], v[140:143], v[156:159]\n"
    "ds_read_b64_tr_b16 v[224:225], v18 offset:45056\n"
    "s_add_u32 m0, s55, 81920\n"
    "ds_read_b64_tr_b16 v[226:227], v18 offset:45568\n"
    "s_mul_i32 s59, s54, 229376\n"
    "s_add_u32 s54, s54, 1\n"
    "s_and_b32 s54, s54, 7\n"
    "buffer_load_dwordx4 v5, s[36:39], s59 offen lds\n"
    "s_waitcnt lgkmcnt(12)\n"
    "v_mfma_f32_16x16x32_bf16 v[164:167], v[204:207], v[140:143], v[164:167]\n"
    "ds_read_b64_tr_b16 v[228:229], v19 offset:45056\n"
    "ds_read_b64_tr_b16 v[230:231], v19 offset:45568\n"
    "v_mfma_f32_16x16x32_bf16 v[160:163], v[204:207], v[136:139], v[160:163]\n"
    "ds_read_b64_tr_b16 v[232:233], v20 offset:45056\n"
    "s_waitcnt lgkmcnt(13)\n"
    "v_mfma_f32_16x16x32_bf16 v[168:171], v[208:211], v[136:139], v[168:171]\n"
    "ds_read_b64_tr_b16 v[234:235], v20 offset:45568\n"
    "ds_read_b64_tr_b16 v[236:237], v21 offset:45056\n"
    "s_add_u32 m0, s55, 90112\n"
    "v_mfma_f32_16x16x32_bf16 v[172:175], v[208:211], v[140:143], v[172:175]\n"
    "buffer_load_dwordx4 v5, s[40:43], s59 offen lds\n"
    "s_waitcnt lgkmcnt(13)\n"
    "v_mfma_f32_16x16x32_bf16 v[180:183], v[212:215], v[140:143], v[180:183]\n"
    "ds_read_b64_tr_b16 v[238:239], v21 offset:45568\n"
    "ds_read_b64_tr_b16 v[240:241], v22 offset:45056\n"
    "v_mfma_f32_16x16x32_bf16 v[176:179], v[212:215], v[136:139], v[176:179]\n"
    "s_waitcnt lgkmcnt(13)\n"
    "v_mfma_f32_16x16x32_bf16 v[184:187], v[216:219], v[136:139], v[184:187]\n"
    "ds_read_b64_tr_b16 v[242:243], v22 offset:45568\n"
    "ds_read_b64_tr_b16 v[244:245], v23 offset:45056\n"
    "v_mfma_f32_16x16x32_bf16 v[188:191], v[216:219], v[140:143], v[188:191]\n"
    "s_waitcnt lgkmcnt(13)\n"
    "v_mfma_f32_16x16x32_bf16 v[196:199], v[220:223], v[140:143], v[196:199]\n"
    "ds_read_b64_tr_b16 v[246:247], v23 offset:45568\n"
    "s_add_u32 m0, s55, 98304\n"
    "v_mfma_f32_16x16x32_bf16 v[192:195], v[220:223], v[136:139], v[192:195]\n"
    "s_mul_i32 s59, s54, 229376\n"
    "s_add_u32 s54, s54, 1\n"
    "s_and_b32 s54, s54, 7\n"
    "buffer_load_dwordx4 v5, s[36:39], s59 offen lds\n"
    "s_waitcnt lgkmcnt(10)\n"
    "v_mfma_f32_16x16x32_bf16 v[152:155], v[224:227], v[144:147], v[152:155]\n"
    "v_mfma_f32_16x16x32_bf16 v[156:159], v[224:227], v[148:151], v[156:159]\n"
    "s_waitcnt lgkmcnt(8)\n"
    "v_mfma_f32_16x16x32_bf16 v[164:167], v[228:231], v[148:151], v[164:167]\n"
    "v_mfma_f32_16x16x32_bf16 v[160:163], v[228:231], v[144:147], v[160:163]\n"
    "s_waitcnt lgkmcnt(0)\n"
    "s_waitcnt vmcnt(3)\n"
    "s_barrier\n"
    "v_mfma_f32_16x16x32_bf16 v[168:171], v[232:235], v[144:147], v[168:171]\n"
    "s_add_u32 m0, s55, 106496\n"
    "v_mfma_f32_16x16x32_bf16 v[172:175], v[232:235], v[148:151], v[172:175]\n"
    "buffer_load_dwordx4 v5, s[40:43], s59 offen lds\n"
    "v_mfma_f32_16x16x32_bf16 v[180:183], v[236:239], v[148:151], v[180:183]\n"
    "v_mfma_f32_16x16x32_bf16 v[176:179], v[236:239], v[144:147], v[176:179]\n"
    "v_mfma_f32_16x16x32_bf16 v[184:187], v[240:243], v[144:147], v[184:187]\n"
    "v_mfma_f32_16x16x32_bf16 v[188:191], v[240:243], v[148:151], v[188:191]\n"
    "v_mfma_f32_16x16x32_bf16 v[196:199], v[244:247], v[148:151], v[196:199]\n"
    "v_mfma_f32_16x16x32_bf16 v[192:195], v[244:247], v[144:147], v[192:195]\n"
    "s_branch Ljoin_2_%=\n"
    "Lvtype_1_%=:\n"
    "s_mov_b32 s67, 2\n"
    "s_waitcnt vmcnt(5)\n"
    "s_barrier\n"
    "ds_read_b128 v[24:27], v16 offset:0\n"
    "ds_read_b128 v[28:31], v16 offset:2048\n"
    "ds_read_b64_tr_b16 v[200:201], v18 offset:0\n"
    "ds_read_b64_tr_b16 v[202:203], v18 offset:512\n"
    "ds_read_b64_tr_b16 v[204:205], v19 offset:0\n"
    "ds_read_b64_tr_b16 v[206:207], v19 offset:512\n"
    "ds_read_b64_tr_b16 v[208:209], v20 offset:0\n"
    "ds_read_b64_tr_b16 v[210:211], v20 offset:512\n"
    "ds_read_b64_tr_b16 v[212:213], v21 offset:0\n"
    "ds_read_b64_tr_b16 v[214:215], v21 offset:512\n"
    "ds_read_b64_tr_b16 v[216:217], v22 offset:0\n"
    "ds_read_b64_tr_b16 v[218:219], v22 offset:512\n"
    "ds_read_b64_tr_b16 v[220:221], v23 offset:0\n"
    "ds_read_b64_tr_b16 v[222:223], v23 offset:512\n"
    "s_lshl_b32 s61, s66, 7\n"
    "s_lshl_b32 s62, s53, 3\n"
    "s_add_u32 s61, s61, s62\n"
    "s_add_u32 s69, s61, s52\n"
    "s_lshl_b32 s61, s69, 10\n"
    "s_lshl_b32 s69, s69, 2\n"
    "s_add_u32 s69, s69, 40960\n"
    "buffer_load_dwordx4 v[136:139], v1, s[24:27], s61 offen\n"
    "global_load_dwordx4 v[140:143], v1, s[16:17]\n"
    "buffer_load_dwordx2 v[148:149], off, s[44:47], 0 offset:0 sc1\n"
    "s_waitcnt lgkmcnt(10)\n"
    "v_mfma_f32_16x16x32_bf16 v[152:155], v[24:27], v[200:203], 0\n"
    "ds_read_b128 v[32:35], v17 offset:0\n"
    "ds_read_b128 v[36:39], v17 offset:2048\n"
    "v_mfma_f32_16x16x32_bf16 v[156:159], v[28:31], v[200:203], 0\n"
    "ds_read_b64_tr_b16 v[224:225], v18 offset:4096\n"
    "ds_read_b64_tr_b16 v[226:227], v18 offset:4608\n"
    "s_lshl_b32 s57, s54, 7\n"
    "s_mul_i32 s58, s54, 196608\n"
    "s_mul_i32 s59, s54, s60\n"
    "s_add_u32 s54, s54, 1\n"
    "s_and_b32 s54, s54, 7\n"
    "s_mul_i32 s61, s53, 11\n"
    "s_lshr_b32 s61, s61, 5\n"
    "s_add_u32 s61, s61, s53\n"
    "s_lshl_b32 s62, s53, 2\n"
    "s_sub_u32 s62, s62, 33\n"
    "s_cmp_lt_u32 s53, 9\n"
    "s_cselect_b32 s20, s61, s62\n"
    "s_mul_i32 s61, s5, 112\n"
    "s_lshl_b32 s62, s71, 1\n"
    "s_add_u32 s61, s61, s62\n"
    "s_add_u32 s61, s61, s4\n"
    "s_lshl_b32 s61, s61, 10\n"
    "s_add_u32 s72, s61, 9437184\n"
    "s_waitcnt lgkmcnt(12)\n"
    "v_mfma_f32_16x16x32_bf16 v[164:167], v[28:31], v[204:207], 0\n"
    "ds_read_b64_tr_b16 v[228:229], v19 offset:4096\n"
    "ds_read_b64_tr_b16 v[230:231], v19 offset:4608\n"
    "v_mfma_f32_16x16x32_bf16 v[160:163], v[24:27], v[204:207], 0\n"
    "s_add_u32 m0, s56, 81920\n"
    "ds_read_b64_tr_b16 v[232:233], v20 offset:4096\n"
    "buffer_load_dwordx4 v2, s[24:27], s57 offen lds\n"
    "s_mul_i32 s61, s5, 112\n"
    "s_lshl_b32 s62, s20, 1\n"
    "s_add_u32 s61, s61, s62\n"
    "s_add_u32 s61, s61, s4\n"
    "s_lshl_b32 s61, s61, 10\n"
    "s_add_u32 s81, s61, 9437184\n"
    "s_lshl_b32 s62, s4, 6\n"
    "v_xor_b32 v252, s62, v13\n"
    "s_cmp_lg_u32 s67, 0\n"
    "s_cselect_b32 s61, 2, 6\n"
    "s_mul_i32 s61, s61, s4\n"
    "s_lshl_b32 s62, s70, 2\n"
    "s_add_u32 s61, s61, s62\n"
    "s_waitcnt lgkmcnt(13)\n"
    "v_mfma_f32_16x16x32_bf16 v[168:171], v[208:211], v[24:27], 0\n"
    "ds_read_b64_tr_b16 v[234:235], v20 offset:4608\n"
    "ds_read_b64_tr_b16 v[236:237], v21 offset:4096\n"
    "v_mfma_f32_16x16x32_bf16 v[172:175], v[208:211], v[28:31], 0\n"
    "s_and_b32 s62, s61, 31\n"
    "s_lshl_b32 s62, s62, 6\n"
    "s_add_u32 s62, s62, s5\n"
    "v_and_b32 v14, 0x23, v7\n"
    "v_and_b32 v12, 12, v7\n"
    "v_lshl_or_b32 v14, v12, 1, v14\n"
    "v_bfe_u32 v12, v7, 4, 1\n"
    "v_lshl_or_b32 v14, v12, 2, v14\n"
    "v_lshlrev_b32 v14, 4, v14\n"
    "s_cmp_lt_u32 s61, 32\n"
    "s_cselect_b32 s50, 0x3eb8aa3b, 1.0\n"
    "s_cselect_b32 s64, 10, 11\n"
    "s_cselect_b64 vcc, -1, 0\n"
    "s_cselect_b32 s61, 1, 3\n"
    "s_waitcnt lgkmcnt(13)\n"
    "v_mfma_f32_16x16x32_bf16 v[180:183], v[212:215], v[28:31], 0\n"
    "ds_read_b64_tr_b16 v[238:239], v21 offset:4608\n"
    "ds_read_b64_tr_b16 v[240:241], v22 offset:4096\n"
    "s_add_u32 m0, s56, 82944\n"
    "v_mfma_f32_16x16x32_bf16 v[176:179], v[212:215], v[24:27], 0\n"
    "buffer_load_dwordx4 v3, s[24:27], s57 offen lds\n"
    "s_mov_b32 s51, s50\n"
    "v_cndmask_b32 v248, v14, v1, vcc\n"
    "s_lshl_b32 s61, s61, 20\n"
    "s_lshl_b32 s62, s62, s64\n"
    "s_add_u32 s70, s61, s62\n"
    "s_lshl_b32 s71, 64, s64\n"
    "s_lshl_b32 s61, s53, 4\n"
    "s_add_u32 s73, s61, s66\n"
    "s_lshl_b32 s73, s73, 7\n"
    "s_add_u32 s73, s73, 655360\n"
    "s_lshl_b32 s61, s20, 7\n"
    "s_and_b32 s62, s66, 7\n"
    "s_mul_i32 s62, s62, 28672\n"
    "s_waitcnt lgkmcnt(13)\n"
    "v_mfma_f32_16x16x32_bf16 v[184:187], v[216:219], v[24:27], 0\n"
    "ds_read_b64_tr_b16 v[242:243], v22 offset:4608\n"
    "ds_read_b64_tr_b16 v[244:245], v23 offset:4096\n"
    "v_mfma_f32_16x16x32_bf16 v[188:191], v[216:219], v[28:31], 0\n"
    "s_add_u32 s74, s61, s62\n"
    "s_lshl_b32 s82, s66, 4\n"
    "s_add_u32 s82, s82, s53\n"
    "s_lshl_b32 s82, s82, 3\n"
    "s_add_u32 s82, s82, s52\n"
    "s_lshl_b32 s82, s82, 3\n"
    "s_add_u32 s82, s82, 8192\n"
    "s_lshl_b32 s6, s79, 2\n"
    "s_lshr_b32 s61, s68, 3\n"
    "s_add_u32 s6, s6, s61\n"
    "s_and_b32 s7, s68, 7\n"
    "s_and_b32 s16, s52, 1\n"
    "s_lshr_b32 s80, s52, 1\n"
    "s_waitcnt lgkmcnt(13)\n"
    "v_mfma_f32_16x16x32_bf16 v[196:199], v[220:223], v[28:31], 0\n"
    "ds_read_b64_tr_b16 v[246:247], v23 offset:4608\n"
    "s_add_u32 m0, s55, 98304\n"
    "v_mfma_f32_16x16x32_bf16 v[192:195], v[220:223], v[24:27], 0\n"
    "buffer_load_dwordx4 v4, s[28:31], s58 offen lds\n"
    "s_lshr_b32 s65, s6, 2\n"
    "s_lshr_b32 s62, s65, 2\n"
    "s_sub_u32 s61, s65, 12\n"
    "s_cmp_lt_u32 s65, 16\n"
    "s_cselect_b32 s61, s62, s61\n"
    "s_mul_i32 s95, s61, 11\n"
    "s_lshr_b32 s95, s95, 5\n"
    "s_add_u32 s61, s61, s95\n"
    "s_add_u32 s61, s61, 1\n"
    "s_lshl_b32 s62, s62, 2\n"
    "s_lshl_b32 s95, 1, s65\n"
    "s_and_b32 s95, s95, 0x7777\n"
    "s_cselect_b32 s62, s62, s61\n"
    "s_waitcnt lgkmcnt(10)\n"
    "v_mfma_f32_16x16x32_bf16 v[152:155], v[32:35], v[224:227], v[152:155]\n"
    "v_mfma_f32_16x16x32_bf16 v[156:159], v[36:39], v[224:227], v[156:159]\n"
    "s_lshl_b32 s62, s62, 4\n"
    "s_add_u32 s65, s65, 8\n"
    "s_lshr_b32 s63, s65, 2\n"
    "s_sub_u32 s61, s65, 12\n"
    "s_cmp_lt_u32 s65, 16\n"
    "s_cselect_b32 s61, s63, s61\n"
    "s_mul_i32 s95, s61, 11\n"
    "s_lshr_b32 s95, s95, 5\n"
    "s_add_u32 s61, s61, s95\n"
    "s_add_u32 s61, s61, 1\n"
    "s_lshl_b32 s63, s63, 2\n"
    "s_lshl_b32 s95, 1, s65\n"
    "s_and_b32 s95, s95, 0x7777\n"
    "s_cselect_b32 s63, s63, s61\n"
    "s_waitcnt lgkmcnt(8)\n"
    "v_mfma_f32_16x16x32_bf16 v[164:167], v[36:39], v[228:231], v[164:167]\n"
    "s_add_u32 m0, s55, 106496\n"
    "v_mfma_f32_16x16x32_bf16 v[160:163], v[32:35], v[228:231], v[160:163]\n"
    "buffer_load_dwordx4 v5, s[36:39], s59 offen lds\n"
    "s_lshl_b32 s63, s63, 4\n"
    "s_add_u32 s65, s65, 8\n"
    "s_lshr_b32 s64, s65, 2\n"
    "s_sub_u32 s61, s65, 12\n"
    "s_cmp_lt_u32 s65, 16\n"
    "s_cselect_b32 s61, s64, s61\n"
    "s_mul_i32 s95, s61, 11\n"
    "s_lshr_b32 s95, s95, 5\n"
    "s_add_u32 s61, s61, s95\n"
    "s_add_u32 s61, s61, 1\n"
    "s_lshl_b32 s64, s64, 2\n"
    "s_lshl_b32 s95, 1, s65\n"
    "s_and_b32 s95, s95, 0x7777\n"
    "s_cselect_b32 s64, s64, s61\n"
    "s_waitcnt lgkmcnt(0)\n"
    "s_waitcnt vmcnt(7)\n"
    "s_barrier\n"
    "v_mfma_f32_16x16x32_bf16 v[168:171], v[232:235], v[32:35], v[168:171]\n"
    "ds_read_b128 v[40:43], v16 offset:40960\n"
    "ds_read_b128 v[44:47], v16 offset:43008\n"
    "ds_read_b64_tr_b16 v[200:201], v18 offset:40960\n"
    "v_mfma_f32_16x16x32_bf16 v[172:175], v[232:235], v[36:39], v[172:175]\n"
    "ds_read_b64_tr_b16 v[202:203], v18 offset:41472\n"
    "ds_read_b64_tr_b16 v[204:205], v19 offset:40960\n"
    "ds_read_b64_tr_b16 v[206:207], v19 offset:41472\n"
    "s_lshl_b32 s64, s64, 4\n"
    "s_lshl_b32 s61, s7, 1\n"
    "s_add_u32 s62, s62, s61\n"
    "v_and_b32 v10, 15, v7\n"
    "v_and_b32 v11, 1, v7\n"
    "v_add_u32 v14, s63, v10\n"
    "v_add_u32 v12, s64, v10\n"
    "v_add_u32 v11, s62, v11\n"
    "v_cmp_le_u32 vcc, 16, v7\n"
    "s_lshl_b32 s61, s6, 6\n"
    "s_lshl_b32 s62, s7, 3\n"
    "v_cndmask_b32 v14, v14, v12, vcc\n"
    "v_cmp_le_u32 vcc, 32, v7\n"
    "v_mfma_f32_16x16x32_bf16 v[180:183], v[236:239], v[36:39], v[180:183]\n"
    "ds_read_b64_tr_b16 v[208:209], v20 offset:40960\n"
    "ds_read_b64_tr_b16 v[210:211], v20 offset:41472\n"
    "ds_read_b64_tr_b16 v[212:213], v21 offset:40960\n"
    "v_mfma_f32_16x16x32_bf16 v[176:179], v[236:239], v[32:35], v[176:179]\n"
    "ds_read_b64_tr_b16 v[214:215], v21 offset:41472\n"
    "ds_read_b64_tr_b16 v[216:217], v22 offset:40960\n"
    "s_add_u32 m0, s55, 114688\n"
    "ds_read_b64_tr_b16 v[218:219], v22 offset:41472\n"
    "buffer_load_dwordx4 v6, s[40:43], s59 offen lds\n"
    "v_mfma_f32_16x16x32_bf16 v[184:187], v[240:243], v[32:35], v[184:187]\n"
    "ds_read_b64_tr_b16 v[220:221], v23 offset:40960\n"
    "ds_read_b64_tr_b16 v[222:223], v23 offset:41472\n"
    "v_mfma_f32_16x16x32_bf16 v[188:191], v[240:243], v[36:39], v[188:191]\n"
    "v_mfma_f32_16x16x32_bf16 v[196:199], v[244:247], v[36:39], v[196:199]\n"
    "v_mfma_f32_16x16x32_bf16 v[192:195], v[244:247], v[32:35], v[192:195]\n"
    "s_waitcnt lgkmcnt(10)\n"
    "v_mfma_f32_16x16x32_bf16 v[152:155], v[40:43], v[200:203], v[152:155]\n"
    "ds_read_b128 v[48:51], v17 offset:40960\n"
    "ds_read_b128 v[52:55], v17 offset:43008\n"
    "v_mfma_f32_16x16x32_bf16 v[156:159], v[44:47], v[200:203], v[156:159]\n"
    "ds_read_b64_tr_b16 v[224:225], v18 offset:45056\n"
    "ds_read_b64_tr_b16 v[226:227], v18 offset:45568\n"
    "s_lshl_b32 s57, s54, 7\n"
    "s_mul_i32 s58, s54, 196608\n"
    "s_mul_i32 s59, s54, s60\n"
    "s_add_u32 s54, s54, 1\n"
    "s_and_b32 s54, s54, 7\n"
    "s_lshl_b32 s63, s16, 2\n"
    "s_add_u32 s62, s62, s63\n"
    "v_cndmask_b32 v14, v14, v11, vcc\n"
    "s_lshl_b32 s63, s80, 6\n"
    "s_lshr_b32 s64, s62, 2\n"
    "s_add_u32 s63, s63, s64\n"
    "v_and_b32 v11, 3, v7\n"
    "v_lshl_add_u32 v11, v11, 4, s63\n"
    "v_cmp_le_u32 vcc, 48, v7\n"
    "s_add_u32 s63, s62, s80\n"
    "s_lshl_b32 s63, s63, 7\n"
    "v_cndmask_b32 v14, v14, v11, vcc\n"
    "s_add_u32 s21, s63, 40960\n"
    "s_waitcnt lgkmcnt(12)\n"
    "v_mfma_f32_16x16x32_bf16 v[164:167], v[44:47], v[204:207], v[164:167]\n"
    "ds_read_b64_tr_b16 v[228:229], v19 offset:45056\n"
    "ds_read_b64_tr_b16 v[230:231], v19 offset:45568\n"
    "v_mfma_f32_16x16x32_bf16 v[160:163], v[40:43], v[204:207], v[160:163]\n"
    "s_add_u32 m0, s56, 122880\n"
    "ds_read_b64_tr_b16 v[232:233], v20 offset:45056\n"
    "buffer_load_dwordx4 v2, s[24:27], s57 offen lds\n"
    "v_lshlrev_b32 v14, 7, v14\n"
    "v_add_u32 v249, 655360, v14\n"
    "v_lshrrev_b32 v11, 5, v7\n"
    "v_lshlrev_b32 v11, 8, v11\n"
    "v_lshl_add_u32 v250, v10, 4, v11\n"
    "s_add_u32 s61, s61, s62\n"
    "s_lshl_b32 s61, s61, 10\n"
    "s_add_u32 s75, s61, 1048576\n"
    "s_lshl_b32 s61, s80, 10\n"
    "s_add_u32 s89, s75, s61\n"
    "s_add_u32 s62, s62, s80\n"
    "s_lshl_b32 s62, s62, 5\n"
    "s_add_u32 s62, s62, s6\n"
    "s_waitcnt lgkmcnt(13)\n"
    "v_mfma_f32_16x16x32_bf16 v[168:171], v[208:211], v[40:43], v[168:171]\n"
    "ds_read_b64_tr_b16 v[234:235], v20 offset:45568\n"
    "ds_read_b64_tr_b16 v[236:237], v21 offset:45056\n"
    "s_add_u32 m0, s56, 123904\n"
    "v_mfma_f32_16x16x32_bf16 v[172:175], v[208:211], v[44:47], v[172:175]\n"
    "buffer_load_dwordx4 v3, s[24:27], s57 offen lds\n"
    "s_lshl_b32 s90, s62, 10\n"
    "s_add_u32 s90, s90, 7340032\n"
    "s_lshl_b32 s91, s62, 7\n"
    "s_add_u32 s91, s91, 786432\n"
    "s_lshl_b32 s76, s6, 17\n"
    "s_add_u32 s76, s76, 3145728\n"
    "s_lshl_b32 s61, s80, 15\n"
    "s_add_u32 s77, s76, s61\n"
    "s_lshl_b32 s78, s7, 12\n"
    "s_lshl_b32 s17, s6, 1\n"
    "s_add_u32 s17, s17, s16\n"
    "s_lshr_b32 s61, s6, 1\n"
    "s_lshl_b32 s61, s61, 10\n"
    "s_waitcnt lgkmcnt(13)\n"
    "v_mfma_f32_16x16x32_bf16 v[180:183], v[212:215], v[44:47], v[180:183]\n"
    "ds_read_b64_tr_b16 v[238:239], v21 offset:45568\n"
    "ds_read_b64_tr_b16 v[240:241], v22 offset:45056\n"
    "s_add_u32 m0, s55, 139264\n"
    "v_mfma_f32_16x16x32_bf16 v[176:179], v[212:215], v[40:43], v[176:179]\n"
    "buffer_load_dwordx4 v4, s[28:31], s58 offen lds\n"
    "s_add_u32 s83, s61, 8192\n"
    "s_lshl_b32 s61, s17, 7\n"
    "s_add_u32 s84, s61, 40960\n"
    "s_lshl_b32 s61, s17, 12\n"
    "s_lshl_b32 s62, s80, 10\n"
    "s_add_u32 s61, s61, s62\n"
    "s_add_u32 s92, s61, 786432\n"
    "s_lshl_b32 s61, s17, 15\n"
    "s_lshl_b32 s62, s80, 13\n"
    "s_add_u32 s93, s61, s62\n"
    "s_add_u32 s93, s93, 7340032\n"
    "s_lshl_b32 s65, s7, 7\n"
    "s_add_u32 s61, s61, s65\n"
    "s_waitcnt lgkmcnt(13)\n"
    "v_mfma_f32_16x16x32_bf16 v[184:187], v[216:219], v[40:43], v[184:187]\n"
    "ds_read_b64_tr_b16 v[242:243], v22 offset:45568\n"
    "ds_read_b64_tr_b16 v[244:245], v23 offset:45056\n"
    "s_add_u32 m0, s55, 147456\n"
    "v_mfma_f32_16x16x32_bf16 v[188:191], v[216:219], v[44:47], v[188:191]\n"
    "buffer_load_dwordx4 v5, s[36:39], s59 offen lds\n"
    "s_add_u32 s94, s61, s62\n"
    "s_mul_i32 s63, s17, 112\n"
    "s_mul_i32 s64, s80, 14\n"
    "s_xor_b32 s61, s6, s7\n"
    "s_add_u32 s62, s61, 0\n"
    "s_and_b32 s62, s62, 1\n"
    "s_mul_i32 s62, s62, 7\n"
    "s_add_u32 s62, s62, s64\n"
    "s_lshl_b32 s96, s62, 15\n"
    "s_add_u32 s62, s62, s63\n"
    "s_lshl_b32 s62, s62, 10\n"
    "s_add_u32 s98, s62, 9437184\n"
    "s_add_u32 s62, s61, 1\n"
    "s_waitcnt lgkmcnt(13)\n"
    "v_mfma_f32_16x16x32_bf16 v[196:199], v[220:223], v[44:47], v[196:199]\n"
    "ds_read_b64_tr_b16 v[246:247], v23 offset:45568\n"
    "v_mfma_f32_16x16x32_bf16 v[192:195], v[220:223], v[40:43], v[192:195]\n"
    "s_and_b32 s62, s62, 1\n"
    "s_mul_i32 s62, s62, 7\n"
    "s_add_u32 s62, s62, s64\n"
    "s_lshl_b32 s97, s62, 15\n"
    "s_add_u32 s62, s62, s63\n"
    "s_lshl_b32 s62, s62, 10\n"
    "s_add_u32 s99, s62, 9437184\n"
    "s_lshl_b32 s61, s16, 11\n"
    "s_mul_i32 s95, s80, 24576\n"
    "s_add_u32 s95, s95, 65536\n"
    "s_add_u32 s87, s95, s61\n"
    "s_lshl_b32 s62, s80, 14\n"
    "s_add_u32 s88, s62, s61\n"
    "s_waitcnt lgkmcnt(10)\n"
    "v_mfma_f32_16x16x32_bf16 v[152:155], v[48:51], v[224:227], v[152:155]\n"
    "s_add_u32 m0, s55, 155648\n"
    "v_mfma_f32_16x16x32_bf16 v[156:159], v[52:55], v[224:227], v[156:159]\n"
    "buffer_load_dwordx4 v6, s[40:43], s59 offen lds\n"
    "s_sub_u32 s95, s95, s62\n"
    "s_and_b32 s61, s6, 3\n"
    "s_lshl_b32 s61, s61, 3\n"
    "s_add_u32 s61, s61, s52\n"
    "s_lshl_b32 s85, s61, 14\n"
    "s_add_u32 s85, s85, s65\n"
    "s_mul_i32 s62, s61, 57344\n"
    "s_add_u32 s86, s62, s65\n"
    "v_lshrrev_b32 v10, 3, v7\n"
    "s_lshl_b32 s62, s16, 4\n"
    "v_add_u32 v10, s62, v10\n"
    "v_lshlrev_b32 v10, 10, v10\n"
    "v_lshrrev_b32 v11, 4, v7\n"
    "s_waitcnt lgkmcnt(8)\n"
    "v_mfma_f32_16x16x32_bf16 v[164:167], v[52:55], v[228:231], v[164:167]\n"
    "v_mfma_f32_16x16x32_bf16 v[160:163], v[48:51], v[228:231], v[160:163]\n"
    "s_lshl_b32 s57, s54, 7\n"
    "s_mul_i32 s58, s54, 196608\n"
    "s_mul_i32 s59, s54, s60\n"
    "s_add_u32 s54, s54, 1\n"
    "s_and_b32 s54, s54, 7\n"
    "v_lshlrev_b32 v11, 1, v11\n"
    "v_and_b32 v12, 7, v7\n"
    "v_xor_b32 v11, v11, v12\n"
    "v_lshl_add_u32 v10, v11, 4, v10\n"
    "v_add_u32 v251, s65, v10\n"
    "v_bfe_u32 v10, v7, 2, 2\n"
    "v_lshrrev_b32 v11, 4, v7\n"
    "v_and_b32 v13, 3, v7\n"
    "v_lshlrev_b32 v13, 3, v13\n"
    "v_lshl_add_u32 v12, v11, 2, v10\n"
    "v_lshl_add_u32 v13, v12, 7, v13\n"
    "v_lshrrev_b32 v10, 1, v10\n"
    "v_and_b32 v12, 1, v11\n"
    "s_waitcnt lgkmcnt(0)\n"
    "s_waitcnt vmcnt(5)\n"
    "s_barrier\n"
    "v_add_u32 v16, 0x14000, v16\n"
    "v_add_u32 v17, 0x14000, v17\n"
    "v_add_u32 v18, 0x14000, v18\n"
    "v_add_u32 v19, 0x14000, v19\n"
    "v_add_u32 v20, 0x14000, v20\n"
    "v_add_u32 v21, 0x14000, v21\n"
    "v_add_u32 v22, 0x14000, v22\n"
    "v_add_u32 v23, 0x14000, v23\n"
    "v_mfma_f32_16x16x32_bf16 v[168:171], v[232:235], v[48:51], v[168:171]\n"
    "ds_read_b128 v[56:59], v16 offset:0\n"
    "ds_read_b128 v[60:63], v16 offset:2048\n"
    "ds_read_b64_tr_b16 v[200:201], v18 offset:0\n"
    "v_mfma_f32_16x16x32_bf16 v[172:175], v[232:235], v[52:55], v[172:175]\n"
    "ds_read_b64_tr_b16 v[202:203], v18 offset:512\n"
    "ds_read_b64_tr_b16 v[204:205], v19 offset:0\n"
    "s_add_u32 m0, s56, 0\n"
    "ds_read_b64_tr_b16 v[206:207], v19 offset:512\n"
    "buffer_load_dwordx4 v2, s[24:27], s57 offen lds\n"
    "v_lshl_or_b32 v10, v12, 1, v10\n"
    "v_lshl_or_b32 v13, v10, 5, v13\n"
    "s_and_b32 s63, s87, 0xfffff000\n"
    "v_add_u32 v253, s63, v13\n"
    "v_lshrrev_b32 v254, 5, v7\n"
    "v_lshlrev_b32 v255, 10, v254\n"
    "v_bfe_u32 v12, v7, 4, 1\n"
    "v_lshl_or_b32 v254, v12, 9, v255\n"
    "v_and_b32 v12, 15, v7\n"
    "v_lshl_or_b32 v254, v12, 4, v254\n"
    "v_mfma_f32_16x16x32_bf16 v[180:183], v[236:239], v[52:55], v[180:183]\n"
    "ds_read_b64_tr_b16 v[208:209], v20 offset:0\n"
    "ds_read_b64_tr_b16 v[210:211], v20 offset:512\n"
    "ds_read_b64_tr_b16 v[212:213], v21 offset:0\n"
    "v_mfma_f32_16x16x32_bf16 v[176:179], v[236:239], v[48:51], v[176:179]\n"
    "ds_read_b64_tr_b16 v[214:215], v21 offset:512\n"
    "ds_read_b64_tr_b16 v[216:217], v22 offset:0\n"
    "s_add_u32 m0, s56, 1024\n"
    "ds_read_b64_tr_b16 v[218:219], v22 offset:512\n"
    "buffer_load_dwordx4 v3, s[24:27], s57 offen lds\n"
    "v_mfma_f32_16x16x32_bf16 v[184:187], v[240:243], v[48:51], v[184:187]\n"
    "ds_read_b64_tr_b16 v[220:221], v23 offset:0\n"
    "ds_read_b64_tr_b16 v[222:223], v23 offset:512\n"
    "v_mfma_f32_16x16x32_bf16 v[188:191], v[240:243], v[52:55], v[188:191]\n"
    "v_mfma_f32_16x16x32_bf16 v[196:199], v[244:247], v[52:55], v[196:199]\n"
    "v_mfma_f32_16x16x32_bf16 v[192:195], v[244:247], v[48:51], v[192:195]\n"
    "s_waitcnt lgkmcnt(10)\n"
    "v_mfma_f32_16x16x32_bf16 v[152:155], v[56:59], v[200:203], v[152:155]\n"
    "ds_read_b128 v[64:67], v17 offset:0\n"
    "ds_read_b128 v[68:71], v17 offset:2048\n"
    "v_mfma_f32_16x16x32_bf16 v[156:159], v[60:63], v[200:203], v[156:159]\n"
    "ds_read_b64_tr_b16 v[224:225], v18 offset:4096\n"
    "s_add_u32 m0, s55, 16384\n"
    "ds_read_b64_tr_b16 v[226:227], v18 offset:4608\n"
    "buffer_load_dwordx4 v4, s[28:31], s58 offen lds\n"
    "s_waitcnt lgkmcnt(12)\n"
    "v_mfma_f32_16x16x32_bf16 v[164:167], v[60:63], v[204:207], v[164:167]\n"
    "ds_read_b64_tr_b16 v[228:229], v19 offset:4096\n"
    "ds_read_b64_tr_b16 v[230:231], v19 offset:4608\n"
    "v_mfma_f32_16x16x32_bf16 v[160:163], v[56:59], v[204:207], v[160:163]\n"
    "s_add_u32 m0, s55, 24576\n"
    "ds_read_b64_tr_b16 v[232:233], v20 offset:4096\n"
    "buffer_load_dwordx4 v5, s[36:39], s59 offen lds\n"
    "s_waitcnt lgkmcnt(13)\n"
    "v_mfma_f32_16x16x32_bf16 v[168:171], v[208:211], v[56:59], v[168:171]\n"
    "ds_read_b64_tr_b16 v[234:235], v20 offset:4608\n"
    "ds_read_b64_tr_b16 v[236:237], v21 offset:4096\n"
    "s_add_u32 m0, s55, 32768\n"
    "v_mfma_f32_16x16x32_bf16 v[172:175], v[208:211], v[60:63], v[172:175]\n"
    "buffer_load_dwordx4 v6, s[40:43], s59 offen lds\n"
    "s_waitcnt lgkmcnt(13)\n"
    "v_mfma_f32_16x16x32_bf16 v[180:183], v[212:215], v[60:63], v[180:183]\n"
    "ds_read_b64_tr_b16 v[238:239], v21 offset:4608\n"
    "ds_read_b64_tr_b16 v[240:241], v22 offset:4096\n"
    "v_mfma_f32_16x16x32_bf16 v[176:179], v[212:215], v[56:59], v[176:179]\n"
    "s_waitcnt lgkmcnt(13)\n"
    "v_mfma_f32_16x16x32_bf16 v[184:187], v[216:219], v[56:59], v[184:187]\n"
    "ds_read_b64_tr_b16 v[242:243], v22 offset:4608\n"
    "ds_read_b64_tr_b16 v[244:245], v23 offset:4096\n"
    "v_mfma_f32_16x16x32_bf16 v[188:191], v[216:219], v[60:63], v[188:191]\n"
    "s_lshl_b32 s57, s54, 7\n"
    "s_mul_i32 s58, s54, 196608\n"
    "s_mul_i32 s59, s54, s60\n"
    "s_add_u32 s54, s54, 1\n"
    "s_and_b32 s54, s54, 7\n"
    "s_waitcnt lgkmcnt(13)\n"
    "v_mfma_f32_16x16x32_bf16 v[196:199], v[220:223], v[60:63], v[196:199]\n"
    "ds_read_b64_tr_b16 v[246:247], v23 offset:4608\n"
    "s_add_u32 m0, s56, 40960\n"
    "v_mfma_f32_16x16x32_bf16 v[192:195], v[220:223], v[56:59], v[192:195]\n"
    "buffer_load_dwordx4 v2, s[24:27], s57 offen lds\n"
    "v_readfirstlane_b32 s22, v148\n"
    "v_readfirstlane_b32 s23, v149\n"
    "v_mov_b32 v14, 0\n"
    "s_nop 1\n"
    "s_add_u32 s22, s22, 1\n"
    "s_addc_u32 s23, s23, 0\n"
    "v_dot2_f32_bf16 v14, v136, v140, v14\n"
    "v_dot2_f32_bf16 v14, v137, v141, v14\n"
    "v_dot2_f32_bf16 v14, v138, v142, v14\n"
    "v_dot2_f32_bf16 v14, v139, v143, v14\n"
    "s_nop 2\n"
    "v_add_f32_dpp v14, v14, v14 row_shr:1 row_mask:0xf bank_mask:0xf bound_ctrl:0\n"
    "s_nop 1\n"
    "v_add_f32_dpp v14, v14, v14 row_shr:2 row_mask:0xf bank_mask:0xf bound_ctrl:0\n"
    "s_nop 1\n"
    "v_add_f32_dpp v14, v14, v14 row_shr:4 row_mask:0xf bank_mask:0xf bound_ctrl:0\n"
    "s_nop 1\n"
    "v_add_f32_dpp v14, v14, v14 row_shr:8 row_mask:0xf bank_mask:0xf bound_ctrl:0\n"
    "s_nop 1\n"
    "v_add_f32_dpp v14, v14, v14 row_bcast:15 row_mask:0xa bank_mask:0xf\n"
    "s_nop 1\n"
    "v_add_f32_dpp v14, v14, v14 row_bcast:31 row_mask:0xc bank_mask:0xf\n"
    "v_mul_f32 v14, 0xbfb8aa3b, v14\n"
    "v_exp_f32 v14, v14\n"
    "v_mov_b32 v13, 0\n"
    "v_add_f32 v14, 1.0, v14\n"
    "v_rcp_f32 v14, v14\n"
    "s_mov_b32 exec_lo, 0\n"
    "s_mov_b32 exec_hi, 0x80000000\n"
    "buffer_store_dword v14, v13, s[44:47], s69 offen sc1\n"
    "s_mov_b64 exec, -1\n"
    "s_waitcnt lgkmcnt(10)\n"
    "v_mfma_f32_16x16x32_bf16 v[152:155], v[64:67], v[224:227], v[152:155]\n"
    "s_add_u32 m0, s56, 41984\n"
    "v_mfma_f32_16x16x32_bf16 v[156:159], v[68:71], v[224:227], v[156:159]\n"
    "buffer_load_dwordx4 v3, s[24:27], s57 offen lds\n"
    "s_waitcnt lgkmcnt(8)\n"
    "v_mfma_f32_16x16x32_bf16 v[164:167], v[68:71], v[228:231], v[164:167]\n"
    "v_mfma_f32_16x16x32_bf16 v[160:163], v[64:67], v[228:231], v[160:163]\n"
    "s_waitcnt lgkmcnt(0)\n"
    "s_waitcnt vmcnt(8)\n"
    "s_barrier\n"
    "v_mfma_f32_16x16x32_bf16 v[168:171], v[232:235], v[64:67], v[168:171]\n"
    "ds_read_b128 v[72:75], v16 offset:40960\n"
    "ds_read_b128 v[76:79], v16 offset:43008\n"
    "ds_read_b64_tr_b16 v[200:201], v18 offset:40960\n"
    "v_mfma_f32_16x16x32_bf16 v[172:175], v[232:235], v[68:71], v[172:175]\n"
    "ds_read_b64_tr_b16 v[202:203], v18 offset:41472\n"
    "ds_read_b64_tr_b16 v[204:205], v19 offset:40960\n"
    "s_add_u32 m0, s55, 57344\n"
    "ds_read_b64_tr_b16 v[206:207], v19 offset:41472\n"
    "buffer_load_dwordx4 v4, s[28:31], s58 offen lds\n"
    "v_mfma_f32_16x16x32_bf16 v[180:183], v[236:239], v[68:71], v[180:183]\n"
    "ds_read_b64_tr_b16 v[208:209], v20 offset:40960\n"
    "ds_read_b64_tr_b16 v[210:211], v20 offset:41472\n"
    "ds_read_b64_tr_b16 v[212:213], v21 offset:40960\n"
    "v_mfma_f32_16x16x32_bf16 v[176:179], v[236:239], v[64:67], v[176:179]\n"
    "ds_read_b64_tr_b16 v[214:215], v21 offset:41472\n"
    "ds_read_b64_tr_b16 v[216:217], v22 offset:40960\n"
    "s_add_u32 m0, s55, 65536\n"
    "ds_read_b64_tr_b16 v[218:219], v22 offset:41472\n"
    "buffer_load_dwordx4 v5, s[36:39], s59 offen lds\n"
    "v_mfma_f32_16x16x32_bf16 v[184:187], v[240:243], v[64:67], v[184:187]\n"
    "ds_read_b64_tr_b16 v[220:221], v23 offset:40960\n"
    "ds_read_b64_tr_b16 v[222:223], v23 offset:41472\n"
    "v_mfma_f32_16x16x32_bf16 v[188:191], v[240:243], v[68:71], v[188:191]\n"
    "v_mfma_f32_16x16x32_bf16 v[196:199], v[244:247], v[68:71], v[196:199]\n"
    "v_mfma_f32_16x16x32_bf16 v[192:195], v[244:247], v[64:67], v[192:195]\n"
    "s_waitcnt lgkmcnt(10)\n"
    "v_mfma_f32_16x16x32_bf16 v[152:155], v[72:75], v[200:203], v[152:155]\n"
    "ds_read_b128 v[80:83], v17 offset:40960\n"
    "ds_read_b128 v[84:87], v17 offset:43008\n"
    "v_mfma_f32_16x16x32_bf16 v[156:159], v[76:79], v[200:203], v[156:159]\n"
    "ds_read_b64_tr_b16 v[224:225], v18 offset:45056\n"
    "s_add_u32 m0, s55, 73728\n"
    "ds_read_b64_tr_b16 v[226:227], v18 offset:45568\n"
    "buffer_load_dwordx4 v6, s[40:43], s59 offen lds\n"
    "s_waitcnt lgkmcnt(12)\n"
    "v_mfma_f32_16x16x32_bf16 v[164:167], v[76:79], v[204:207], v[164:167]\n"
    "ds_read_b64_tr_b16 v[228:229], v19 offset:45056\n"
    "ds_read_b64_tr_b16 v[230:231], v19 offset:45568\n"
    "v_mfma_f32_16x16x32_bf16 v[160:163], v[72:75], v[204:207], v[160:163]\n"
    "ds_read_b64_tr_b16 v[232:233], v20 offset:45056\n"
    "s_lshl_b32 s57, s54, 7\n"
    "s_mul_i32 s58, s54, 196608\n"
    "s_mul_i32 s59, s54, s60\n"
    "s_add_u32 s54, s54, 1\n"
    "s_and_b32 s54, s54, 7\n"
    "s_waitcnt lgkmcnt(13)\n"
    "v_mfma_f32_16x16x32_bf16 v[168:171], v[208:211], v[72:75], v[168:171]\n"
    "ds_read_b64_tr_b16 v[234:235], v20 offset:45568\n"
    "ds_read_b64_tr_b16 v[236:237], v21 offset:45056\n"
    "s_add_u32 m0, s56, 81920\n"
    "v_mfma_f32_16x16x32_bf16 v[172:175], v[208:211], v[76:79], v[172:175]\n"
    "buffer_load_dwordx4 v2, s[24:27], s57 offen lds\n"
    "s_waitcnt lgkmcnt(13)\n"
    "v_mfma_f32_16x16x32_bf16 v[180:183], v[212:215], v[76:79], v[180:183]\n"
    "ds_read_b64_tr_b16 v[238:239], v21 offset:45568\n"
    "ds_read_b64_tr_b16 v[240:241], v22 offset:45056\n"
    "v_mfma_f32_16x16x32_bf16 v[176:179], v[212:215], v[72:75], v[176:179]\n"
    "s_waitcnt lgkmcnt(13)\n"
    "v_mfma_f32_16x16x32_bf16 v[184:187], v[216:219], v[72:75], v[184:187]\n"
    "ds_read_b64_tr_b16 v[242:243], v22 offset:45568\n"
    "ds_read_b64_tr_b16 v[244:245], v23 offset:45056\n"
    "s_add_u32 m0, s56, 82944\n"
    "v_mfma_f32_16x16x32_bf16 v[188:191], v[216:219], v[76:79], v[188:191]\n"
    "buffer_load_dwordx4 v3, s[24:27], s57 offen lds\n"
    "s_waitcnt lgkmcnt(13)\n"
    "v_mfma_f32_16x16x32_bf16 v[196:199], v[220:223], v[76:79], v[196:199]\n"
    "ds_read_b64_tr_b16 v[246:247], v23 offset:45568\n"
    "s_add_u32 m0, s55, 98304\n"
    "v_mfma_f32_16x16x32_bf16 v[192:195], v[220:223], v[72:75], v[192:195]\n"
    "buffer_load_dwordx4 v4, s[28:31], s58 offen lds\n"
    "s_waitcnt lgkmcnt(10)\n"
    "v_mfma_f32_16x16x32_bf16 v[152:155], v[80:83], v[224:227], v[152:155]\n"
    "s_add_u32 m0, s55, 106496\n"
    "v_mfma_f32_16x16x32_bf16 v[156:159], v[84:87], v[224:227], v[156:159]\n"
    "buffer_load_dwordx4 v5, s[36:39], s59 offen lds\n"
    "s_waitcnt lgkmcnt(8)\n"
    "v_mfma_f32_16x16x32_bf16 v[164:167], v[84:87], v[228:231], v[164:167]\n"
    "v_mfma_f32_16x16x32_bf16 v[160:163], v[80:83], v[228:231], v[160:163]\n"
    "s_waitcnt lgkmcnt(0)\n"
    "s_waitcnt vmcnt(10)\n"
    "s_barrier\n"
    "v_add_u32 v16, 0xfffec000, v16\n"
    "v_add_u32 v17, 0xfffec000, v17\n"
    "v_add_u32 v18, 0xfffec000, v18\n"
    "v_add_u32 v19, 0xfffec000, v19\n"
    "v_add_u32 v20, 0xfffec000, v20\n"
    "v_add_u32 v21, 0xfffec000, v21\n"
    "v_add_u32 v22, 0xfffec000, v22\n"
    "v_add_u32 v23, 0xfffec000, v23\n"
    "v_mfma_f32_16x16x32_bf16 v[168:171], v[232:235], v[80:83], v[168:171]\n"
    "ds_read_b128 v[88:91], v16 offset:0\n"
    "ds_read_b128 v[92:95], v16 offset:2048\n"
    "ds_read_b64_tr_b16 v[200:201], v18 offset:0\n"
    "v_mfma_f32_16x16x32_bf16 v[172:175], v[232:235], v[84:87], v[172:175]\n"
    "ds_read_b64_tr_b16 v[202:203], v18 offset:512\n"
    "ds_read_b64_tr_b16 v[204:205], v19 offset:0\n"
    "s_add_u32 m0, s55, 114688\n"
    "ds_read_b64_tr_b16 v[206:207], v19 offset:512\n"
    "buffer_load_dwordx4 v6, s[40:43], s59 offen lds\n"
    "v_mfma_f32_16x16x32_bf16 v[180:183], v[236:239], v[84:87], v[180:183]\n"
    "ds_read_b64_tr_b16 v[208:209], v20 offset:0\n"
    "ds_read_b64_tr_b16 v[210:211], v20 offset:512\n"
    "ds_read_b64_tr_b16 v[212:213], v21 offset:0\n"
    "v_mfma_f32_16x16x32_bf16 v[176:179], v[236:239], v[80:83], v[176:179]\n"
    "ds_read_b64_tr_b16 v[214:215], v21 offset:512\n"
    "ds_read_b64_tr_b16 v[216:217], v22 offset:0\n"
    "ds_read_b64_tr_b16 v[218:219], v22 offset:512\n"
    "s_bitcmp1_b32 s52, 0\n"
    "s_cselect_b64 s[48:49], s[12:13], s[10:11]\n"
    "s_lshr_b32 s61, s52, 1\n"
    "s_add_u32 s61, s61, s54\n"
    "s_add_u32 s61, s61, 1\n"
    "s_and_b32 s61, s61, 7\n"
    "s_mul_i32 s61, s61, 229376\n"
    "s_add_u32 s61, s61, s74\n"
    "v_lshrrev_b32 v10, 3, v7\n"
    "v_mul_u32_u24 v10, 3584, v10\n"
    "v_and_b32 v11, 0x70, v1\n"
    "v_add3_u32 v10, v10, v11, s61\n"
    "global_load_dwordx4 v[144:147], v10, s[48:49]\n"
    "v_mfma_f32_16x16x32_bf16 v[184:187], v[240:243], v[80:83], v[184:187]\n"
    "ds_read_b64_tr_b16 v[220:221], v23 offset:0\n"
    "ds_read_b64_tr_b16 v[222:223], v23 offset:512\n"
    "v_mfma_f32_16x16x32_bf16 v[188:191], v[240:243], v[84:87], v[188:191]\n"
    "v_mfma_f32_16x16x32_bf16 v[196:199], v[244:247], v[84:87], v[196:199]\n"
    "v_mfma_f32_16x16x32_bf16 v[192:195], v[244:247], v[80:83], v[192:195]\n"
    "s_waitcnt lgkmcnt(10)\n"
    "v_mfma_f32_16x16x32_bf16 v[152:155], v[88:91], v[200:203], v[152:155]\n"
    "ds_read_b128 v[96:99], v17 offset:0\n"
    "ds_read_b128 v[100:103], v17 offset:2048\n"
    "v_mfma_f32_16x16x32_bf16 v[156:159], v[92:95], v[200:203], v[156:159]\n"
    "ds_read_b64_tr_b16 v[224:225], v18 offset:4096\n"
    "ds_read_b64_tr_b16 v[226:227], v18 offset:4608\n"
    "s_lshl_b32 s57, s54, 7\n"
    "s_mul_i32 s58, s54, 196608\n"
    "s_mul_i32 s59, s54, s60\n"
    "s_add_u32 s54, s54, 1\n"
    "s_and_b32 s54, s54, 7\n"
    "s_waitcnt lgkmcnt(12)\n"
    "v_mfma_f32_16x16x32_bf16 v[164:167], v[92:95], v[204:207], v[164:167]\n"
    "ds_read_b64_tr_b16 v[228:229], v19 offset:4096\n"
    "ds_read_b64_tr_b16 v[230:231], v19 offset:4608\n"
    "v_mfma_f32_16x16x32_bf16 v[160:163], v[88:91], v[204:207], v[160:163]\n"
    "s_add_u32 m0, s56, 122880\n"
    "ds_read_b64_tr_b16 v[232:233], v20 offset:4096\n"
    "buffer_load_dwordx4 v2, s[24:27], s57 offen lds\n"
    "s_waitcnt lgkmcnt(13)\n"
    "v_mfma_f32_16x16x32_bf16 v[168:171], v[208:211], v[88:91], v[168:171]\n"
    "ds_read_b64_tr_b16 v[234:235], v20 offset:4608\n"
    "ds_read_b64_tr_b16 v[236:237], v21 offset:4096\n"
    "v_mfma_f32_16x16x32_bf16 v[172:175], v[208:211], v[92:95], v[172:175]\n"
    "s_waitcnt lgkmcnt(13)\n"
    "v_mfma_f32_16x16x32_bf16 v[180:183], v[212:215], v[92:95], v[180:183]\n"
    "ds_read_b64_tr_b16 v[238:239], v21 offset:4608\n"
    "ds_read_b64_tr_b16 v[240:241], v22 offset:4096\n"
    "s_add_u32 m0, s56, 123904\n"
    "v_mfma_f32_16x16x32_bf16 v[176:179], v[212:215], v[88:91], v[176:179]\n"
    "buffer_load_dwordx4 v3, s[24:27], s57 offen lds\n"
    "s_waitcnt lgkmcnt(13)\n"
    "v_mfma_f32_16x16x32_bf16 v[184:187], v[216:219], v[88:91], v[184:187]\n"
    "ds_read_b64_tr_b16 v[242:243], v22 offset:4608\n"
    "ds_read_b64_tr_b16 v[244:245], v23 offset:4096\n"
    "v_mfma_f32_16x16x32_bf16 v[188:191], v[216:219], v[92:95], v[188:191]\n"
    "s_waitcnt lgkmcnt(13)\n"
    "v_mfma_f32_16x16x32_bf16 v[196:199], v[220:223], v[92:95], v[196:199]\n"
    "ds_read_b64_tr_b16 v[246:247], v23 offset:4608\n"
    "s_add_u32 m0, s55, 139264\n"
    "v_mfma_f32_16x16x32_bf16 v[192:195], v[220:223], v[88:91], v[192:195]\n"
    "buffer_load_dwordx4 v4, s[28:31], s58 offen lds\n"
    "s_waitcnt lgkmcnt(10)\n"
    "v_mfma_f32_16x16x32_bf16 v[152:155], v[96:99], v[224:227], v[152:155]\n"
    "v_mfma_f32_16x16x32_bf16 v[156:159], v[100:103], v[224:227], v[156:159]\n"
    "s_waitcnt lgkmcnt(8)\n"
    "v_mfma_f32_16x16x32_bf16 v[164:167], v[100:103], v[228:231], v[164:167]\n"
    "s_add_u32 m0, s55, 147456\n"
    "v_mfma_f32_16x16x32_bf16 v[160:163], v[96:99], v[228:231], v[160:163]\n"
    "buffer_load_dwordx4 v5, s[36:39], s59 offen lds\n"
    "s_waitcnt lgkmcnt(0)\n"
    "s_waitcnt vmcnt(10)\n"
    "s_barrier\n"
    "v_mfma_f32_16x16x32_bf16 v[168:171], v[232:235], v[96:99], v[168:171]\n"
    "ds_read_b128 v[104:107], v16 offset:40960\n"
    "ds_read_b128 v[108:111], v16 offset:43008\n"
    "ds_read_b64_tr_b16 v[200:201], v18 offset:40960\n"
    "v_mfma_f32_16x16x32_bf16 v[172:175], v[232:235], v[100:103], v[172:175]\n"
    "ds_read_b64_tr_b16 v[202:203], v18 offset:41472\n"
    "ds_read_b64_tr_b16 v[204:205], v19 offset:40960\n"
    "ds_read_b64_tr_b16 v[206:207], v19 offset:41472\n"
    "v_mfma_f32_16x16x32_bf16 v[180:183], v[236:239], v[100:103], v[180:183]\n"
    "ds_read_b64_tr_b16 v[208:209], v20 offset:40960\n"
    "ds_read_b64_tr_b16 v[210:211], v20 offset:41472\n"
    "ds_read_b64_tr_b16 v[212:213], v21 offset:40960\n"
    "v_mfma_f32_16x16x32_bf16 v[176:179], v[236:239], v[96:99], v[176:179]\n"
    "ds_read_b64_tr_b16 v[214:215], v21 offset:41472\n"
    "ds_read_b64_tr_b16 v[216:217], v22 offset:40960\n"
    "s_add_u32 m0, s55, 155648\n"
    "ds_read_b64_tr_b16 v[218:219], v22 offset:41472\n"
    "buffer_load_dwordx4 v6, s[40:43], s59 offen lds\n"
    "v_mfma_f32_16x16x32_bf16 v[184:187], v[240:243], v[96:99], v[184:187]\n"
    "ds_read_b64_tr_b16 v[220:221], v23 offset:40960\n"
    "ds_read_b64_tr_b16 v[222:223], v23 offset:41472\n"
    "v_mfma_f32_16x16x32_bf16 v[188:191], v[240:243], v[100:103], v[188:191]\n"
    "v_mfma_f32_16x16x32_bf16 v[196:199], v[244:247], v[100:103], v[196:199]\n"
    "v_mfma_f32_16x16x32_bf16 v[192:195], v[244:247], v[96:99], v[192:195]\n"
    "s_waitcnt lgkmcnt(10)\n"
    "v_mfma_f32_16x16x32_bf16 v[152:155], v[104:107], v[200:203], v[152:155]\n"
    "ds_read_b128 v[112:115], v17 offset:40960\n"
    "ds_read_b128 v[116:119], v17 offset:43008\n"
    "v_mfma_f32_16x16x32_bf16 v[156:159], v[108:111], v[200:203], v[156:159]\n"
    "ds_read_b64_tr_b16 v[224:225], v18 offset:45056\n"
    "ds_read_b64_tr_b16 v[226:227], v18 offset:45568\n"
    "s_mul_i32 s61, %2, 7\n"
    "s_add_u32 s61, s61, s52\n"
    "s_lshl_b32 s62, %2, 1\n"
    "s_cmp_eq_u32 s52, 7\n"
    "s_cselect_b64 s[48:49], s[8:9], s[14:15]\n"
    "s_cselect_b32 s61, s62, s61\n"
    "s_cselect_b32 s62, 15, 7\n"
    "s_lshl_b32 s61, s61, 10\n"
    "v_and_b32 v10, s62, v7\n"
    "v_lshl_add_u32 v10, v10, 7, s61\n"
    "global_load_dword v9, v10, s[48:49]\n"
    "s_waitcnt lgkmcnt(12)\n"
    "v_mfma_f32_16x16x32_bf16 v[164:167], v[108:111], v[204:207], v[164:167]\n"
    "ds_read_b64_tr_b16 v[228:229], v19 offset:45056\n"
    "ds_read_b64_tr_b16 v[230:231], v19 offset:45568\n"
    "v_mfma_f32_16x16x32_bf16 v[160:163], v[104:107], v[204:207], v[160:163]\n"
    "ds_read_b64_tr_b16 v[232:233], v20 offset:45056\n"
    "s_mov_b64 s[36:37], s[10:11]\n"
    "s_mov_b32 s38, 1835008\n"
    "s_mov_b32 s39, 0x20000\n"
    "s_mov_b64 s[40:41], s[12:13]\n"
    "s_mov_b32 s42, 1835008\n"
    "s_mov_b32 s43, 0x20000\n"
    "v_lshrrev_b32 v10, 3, v7\n"
    "s_lshl_b32 s61, s52, 3\n"
    "v_add_u32 v10, s61, v10\n"
    "v_mul_u32_u24 v10, 3584, v10\n"
    "v_and_b32 v5, 0x70, v4\n"
    "s_lshl_b32 s61, s20, 7\n"
    "v_add3_u32 v5, v5, v10, s61\n"
    "s_waitcnt lgkmcnt(13)\n"
    "v_mfma_f32_16x16x32_bf16 v[168:171], v[208:211], v[104:107], v[168:171]\n"
    "ds_read_b64_tr_b16 v[234:235], v20 offset:45568\n"
    "ds_read_b64_tr_b16 v[236:237], v21 offset:45056\n"
    "v_mfma_f32_16x16x32_bf16 v[172:175], v[208:211], v[108:111], v[172:175]\n"
    "s_waitcnt lgkmcnt(13)\n"
    "v_mfma_f32_16x16x32_bf16 v[180:183], v[212:215], v[108:111], v[180:183]\n"
    "ds_read_b64_tr_b16 v[238:239], v21 offset:45568\n"
    "ds_read_b64_tr_b16 v[240:241], v22 offset:45056\n"
    "s_add_u32 m0, s55, 0\n"
    "v_mfma_f32_16x16x32_bf16 v[176:179], v[212:215], v[104:107], v[176:179]\n"
    "s_mul_i32 s59, s54, 229376\n"
    "s_add_u32 s54, s54, 1\n"
    "s_and_b32 s54, s54, 7\n"
    "buffer_load_dwordx4 v5, s[36:39], s59 offen lds\n"
    "s_waitcnt lgkmcnt(13)\n"
    "v_mfma_f32_16x16x32_bf16 v[184:187], v[216:219], v[104:107], v[184:187]\n"
    "ds_read_b64_tr_b16 v[242:243], v22 offset:45568\n"
    "ds_read_b64_tr_b16 v[244:245], v23 offset:45056\n"
    "v_mfma_f32_16x16x32_bf16 v[188:191], v[216:219], v[108:111], v[188:191]\n"
    "s_waitcnt lgkmcnt(13)\n"
    "v_mfma_f32_16x16x32_bf16 v[196:199], v[220:223], v[108:111], v[196:199]\n"
    "ds_read_b64_tr_b16 v[246:247], v23 offset:45568\n"
    "s_add_u32 m0, s55, 8192\n"
    "v_mfma_f32_16x16x32_bf16 v[192:195], v[220:223], v[104:107], v[192:195]\n"
    "buffer_load_dwordx4 v5, s[40:43], s59 offen lds\n"
    "s_waitcnt lgkmcnt(10)\n"
    "v_mfma_f32_16x16x32_bf16 v[152:155], v[112:115], v[224:227], v[152:155]\n"
    "v_mfma_f32_16x16x32_bf16 v[156:159], v[116:119], v[224:227], v[156:159]\n"
    "s_waitcnt lgkmcnt(8)\n"
    "v_mfma_f32_16x16x32_bf16 v[164:167], v[116:119], v[228:231], v[164:167]\n"
    "s_add_u32 m0, s55, 16384\n"
    "v_mfma_f32_16x16x32_bf16 v[160:163], v[112:115], v[228:231], v[160:163]\n"
    "s_mul_i32 s59, s54, 229376\n"
    "s_add_u32 s54, s54, 1\n"
    "s_and_b32 s54, s54, 7\n"
    "buffer_load_dwordx4 v5, s[36:39], s59 offen lds\n"
    "s_waitcnt lgkmcnt(0)\n"
    "s_waitcnt vmcnt(10)\n"
    "s_barrier\n"
    "v_add_u32 v16, 0x14000, v16\n"
    "v_add_u32 v17, 0x14000, v17\n"
    "v_add_u32 v18, 0x14000, v18\n"
    "v_add_u32 v19, 0x14000, v19\n"
    "v_add_u32 v20, 0x14000, v20\n"
    "v_add_u32 v21, 0x14000, v21\n"
    "v_add_u32 v22, 0x14000, v22\n"
    "v_add_u32 v23, 0x14000, v23\n"
    "v_mfma_f32_16x16x32_bf16 v[168:171], v[232:235], v[112:115], v[168:171]\n"
    "ds_read_b128 v[120:123], v16 offset:0\n"
    "ds_read_b128 v[124:127], v16 offset:2048\n"
    "ds_read_b64_tr_b16 v[200:201], v18 offset:0\n"
    "v_mfma_f32_16x16x32_bf16 v[172:175], v[232:235], v[116:119], v[172:175]\n"
    "ds_read_b64_tr_b16 v[202:203], v18 offset:512\n"
    "ds_read_b64_tr_b16 v[204:205], v19 offset:0\n"
    "ds_read_b64_tr_b16 v[206:207], v19 offset:512\n"
    "v_mfma_f32_16x16x32_bf16 v[180:183], v[236:239], v[116:119], v[180:183]\n"
    "ds_read_b64_tr_b16 v[208:209], v20 offset:0\n"
    "ds_read_b64_tr_b16 v[210:211], v20 offset:512\n"
    "ds_read_b64_tr_b16 v[212:213], v21 offset:0\n"
    "v_mfma_f32_16x16x32_bf16 v[176:179], v[236:239], v[112:115], v[176:179]\n"
    "ds_read_b64_tr_b16 v[214:215], v21 offset:512\n"
    "ds_read_b64_tr_b16 v[216:217], v22 offset:0\n"
    "s_add_u32 m0, s55, 24576\n"
    "ds_read_b64_tr_b16 v[218:219], v22 offset:512\n"
    "buffer_load_dwordx4 v5, s[40:43], s59 offen lds\n"
    "v_mfma_f32_16x16x32_bf16 v[184:187], v[240:243], v[112:115], v[184:187]\n"
    "ds_read_b64_tr_b16 v[220:221], v23 offset:0\n"
    "ds_read_b64_tr_b16 v[222:223], v23 offset:512\n"
    "v_mfma_f32_16x16x32_bf16 v[188:191], v[240:243], v[116:119], v[188:191]\n"
    "v_mfma_f32_16x16x32_bf16 v[196:199], v[244:247], v[116:119], v[196:199]\n"
    "v_mfma_f32_16x16x32_bf16 v[192:195], v[244:247], v[112:115], v[192:195]\n"
    "s_waitcnt lgkmcnt(10)\n"
    "v_mfma_f32_16x16x32_bf16 v[152:155], v[120:123], v[200:203], v[152:155]\n"
    "ds_read_b128 v[128:131], v17 offset:0\n"
    "ds_read_b128 v[132:135], v17 offset:2048\n"
    "v_mfma_f32_16x16x32_bf16 v[156:159], v[124:127], v[200:203], v[156:159]\n"
    "ds_read_b64_tr_b16 v[224:225], v18 offset:4096\n"
    "s_add_u32 m0, s55, 40960\n"
    "ds_read_b64_tr_b16 v[226:227], v18 offset:4608\n"
    "s_mul_i32 s59, s54, 229376\n"
    "s_add_u32 s54, s54, 1\n"
    "s_and_b32 s54, s54, 7\n"
    "buffer_load_dwordx4 v5, s[36:39], s59 offen lds\n"
    "s_waitcnt lgkmcnt(12)\n"
    "v_mfma_f32_16x16x32_bf16 v[164:167], v[124:127], v[204:207], v[164:167]\n"
    "ds_read_b64_tr_b16 v[228:229], v19 offset:4096\n"
    "ds_read_b64_tr_b16 v[230:231], v19 offset:4608\n"
    "v_mfma_f32_16x16x32_bf16 v[160:163], v[120:123], v[204:207], v[160:163]\n"
    "ds_read_b64_tr_b16 v[232:233], v20 offset:4096\n"
    "s_waitcnt lgkmcnt(13)\n"
    "v_mfma_f32_16x16x32_bf16 v[168:171], v[208:211], v[120:123], v[168:171]\n"
    "ds_read_b64_tr_b16 v[234:235], v20 offset:4608\n"
    "ds_read_b64_tr_b16 v[236:237], v21 offset:4096\n"
    "s_add_u32 m0, s55, 49152\n"
    "v_mfma_f32_16x16x32_bf16 v[172:175], v[208:211], v[124:127], v[172:175]\n"
    "buffer_load_dwordx4 v5, s[40:43], s59 offen lds\n"
    "s_waitcnt lgkmcnt(13)\n"
    "v_mfma_f32_16x16x32_bf16 v[180:183], v[212:215], v[124:127], v[180:183]\n"
    "ds_read_b64_tr_b16 v[238:239], v21 offset:4608\n"
    "ds_read_b64_tr_b16 v[240:241], v22 offset:4096\n"
    "v_mfma_f32_16x16x32_bf16 v[176:179], v[212:215], v[120:123], v[176:179]\n"
    "s_waitcnt lgkmcnt(13)\n"
    "v_mfma_f32_16x16x32_bf16 v[184:187], v[216:219], v[120:123], v[184:187]\n"
    "ds_read_b64_tr_b16 v[242:243], v22 offset:4608\n"
    "ds_read_b64_tr_b16 v[244:245], v23 offset:4096\n"
    "v_mfma_f32_16x16x32_bf16 v[188:191], v[216:219], v[124:127], v[188:191]\n"
    "s_waitcnt lgkmcnt(13)\n"
    "v_mfma_f32_16x16x32_bf16 v[196:199], v[220:223], v[124:127], v[196:199]\n"
    "ds_read_b64_tr_b16 v[246:247], v23 offset:4608\n"
    "s_add_u32 m0, s55, 57344\n"
    "v_mfma_f32_16x16x32_bf16 v[192:195], v[220:223], v[120:123], v[192:195]\n"
    "s_mul_i32 s59, s54, 229376\n"
    "s_add_u32 s54, s54, 1\n"
    "s_and_b32 s54, s54, 7\n"
    "buffer_load_dwordx4 v5, s[36:39], s59 offen lds\n"
    "s_waitcnt lgkmcnt(10)\n"
    "v_mfma_f32_16x16x32_bf16 v[152:155], v[128:131], v[224:227], v[152:155]\n"
    "v_mfma_f32_16x16x32_bf16 v[156:159], v[132:135], v[224:227], v[156:159]\n"
    "s_waitcnt lgkmcnt(8)\n"
    "v_mfma_f32_16x16x32_bf16 v[164:167], v[132:135], v[228:231], v[164:167]\n"
    "v_mfma_f32_16x16x32_bf16 v[160:163], v[128:131], v[228:231], v[160:163]\n"
    "s_waitcnt lgkmcnt(0)\n"
    "s_waitcnt vmcnt(8)\n"
    "s_barrier\n"
    "v_mfma_f32_16x16x32_bf16 v[168:171], v[232:235], v[128:131], v[168:171]\n"
    "ds_read_b128 v[136:139], v16 offset:40960\n"
    "ds_read_b128 v[140:143], v16 offset:43008\n"
    "ds_read_b64_tr_b16 v[200:201], v18 offset:40960\n"
    "v_mfma_f32_16x16x32_bf16 v[172:175], v[232:235], v[132:135], v[172:175]\n"
    "ds_read_b64_tr_b16 v[202:203], v18 offset:41472\n"
    "ds_read_b64_tr_b16 v[204:205], v19 offset:40960\n"
    "s_add_u32 m0, s55, 65536\n"
    "ds_read_b64_tr_b16 v[206:207], v19 offset:41472\n"
    "buffer_load_dwordx4 v5, s[40:43], s59 offen lds\n"
    "v_mfma_f32_16x16x32_bf16 v[180:183], v[236:239], v[132:135], v[180:183]\n"
    "ds_read_b64_tr_b16 v[208:209], v20 offset:40960\n"
    "ds_read_b64_tr_b16 v[210:211], v20 offset:41472\n"
    "ds_read_b64_tr_b16 v[212:213], v21 offset:40960\n"
    "v_mfma_f32_16x16x32_bf16 v[176:179], v[236:239], v[128:131], v[176:179]\n"
    "ds_read_b64_tr_b16 v[214:215], v21 offset:41472\n"
    "ds_read_b64_tr_b16 v[216:217], v22 offset:40960\n"
    "ds_read_b64_tr_b16 v[218:219], v22 offset:41472\n"
    "v_mfma_f32_16x16x32_bf16 v[184:187], v[240:243], v[128:131], v[184:187]\n"
    "ds_read_b64_tr_b16 v[220:221], v23 offset:40960\n"
    "ds_read_b64_tr_b16 v[222:223], v23 offset:41472\n"
    "v_mfma_f32_16x16x32_bf16 v[188:191], v[240:243], v[132:135], v[188:191]\n"
    "v_mfma_f32_16x16x32_bf16 v[196:199], v[244:247], v[132:135], v[196:199]\n"
    "v_mfma_f32_16x16x32_bf16 v[192:195], v[244:247], v[128:131], v[192:195]\n"
    "s_waitcnt lgkmcnt(10)\n"
    "v_mfma_f32_16x16x32_bf16 v[152:155], v[136:139], v[200:203], v[152:155]\n"
    "ds_read_b128 v[144:147], v17 offset:40960\n"
    "ds_read_b128 v[148:151], v17 offset:43008\n"
    "v_mfma_f32_16x16x32_bf16 v[156:159], v[140:143], v[200:203], v[156:159]\n"
    "ds_read_b64_tr_b16 v[224:225], v18 offset:45056\n"
    "s_add_u32 m0, s55, 81920\n"
    "ds_read_b64_tr_b16 v[226:227], v18 offset:45568\n"
    "s_mul_i32 s59, s54, 229376\n"
    "s_add_u32 s54, s54, 1\n"
    "s_and_b32 s54, s54, 7\n"
    "buffer_load_dwordx4 v5, s[36:39], s59 offen lds\n"
    "s_waitcnt lgkmcnt(12)\n"
    "v_mfma_f32_16x16x32_bf16 v[164:167], v[140:143], v[204:207], v[164:167]\n"
    "ds_read_b64_tr_b16 v[228:229], v19 offset:45056\n"
    "ds_read_b64_tr_b16 v[230:231], v19 offset:45568\n"
    "v_mfma_f32_16x16x32_bf16 v[160:163], v[136:139], v[204:207], v[160:163]\n"
    "ds_read_b64_tr_b16 v[232:233], v20 offset:45056\n"
    "s_waitcnt lgkmcnt(13)\n"
    "v_mfma_f32_16x16x32_bf16 v[168:171], v[208:211], v[136:139], v[168:171]\n"
    "ds_read_b64_tr_b16 v[234:235], v20 offset:45568\n"
    "ds_read_b64_tr_b16 v[236:237], v21 offset:45056\n"
    "s_add_u32 m0, s55, 90112\n"
    "v_mfma_f32_16x16x32_bf16 v[172:175], v[208:211], v[140:143], v[172:175]\n"
    "buffer_load_dwordx4 v5, s[40:43], s59 offen lds\n"
    "s_waitcnt lgkmcnt(13)\n"
    "v_mfma_f32_16x16x32_bf16 v[180:183], v[212:215], v[140:143], v[180:183]\n"
    "ds_read_b64_tr_b16 v[238:239], v21 offset:45568\n"
    "ds_read_b64_tr_b16 v[240:241], v22 offset:45056\n"
    "v_mfma_f32_16x16x32_bf16 v[176:179], v[212:215], v[136:139], v[176:179]\n"
    "s_waitcnt lgkmcnt(13)\n"
    "v_mfma_f32_16x16x32_bf16 v[184:187], v[216:219], v[136:139], v[184:187]\n"
    "ds_read_b64_tr_b16 v[242:243], v22 offset:45568\n"
    "ds_read_b64_tr_b16 v[244:245], v23 offset:45056\n"
    "v_mfma_f32_16x16x32_bf16 v[188:191], v[216:219], v[140:143], v[188:191]\n"
    "s_waitcnt lgkmcnt(13)\n"
    "v_mfma_f32_16x16x32_bf16 v[196:199], v[220:223], v[140:143], v[196:199]\n"
    "ds_read_b64_tr_b16 v[246:247], v23 offset:45568\n"
    "s_add_u32 m0, s55, 98304\n"
    "v_mfma_f32_16x16x32_bf16 v[192:195], v[220:223], v[136:139], v[192:195]\n"
    "s_mul_i32 s59, s54, 229376\n"
    "s_add_u32 s54, s54, 1\n"
    "s_and_b32 s54, s54, 7\n"
    "buffer_load_dwordx4 v5, s[36:39], s59 offen lds\n"
    "s_waitcnt lgkmcnt(10)\n"
    "v_mfma_f32_16x16x32_bf16 v[152:155], v[144:147], v[224:227], v[152:155]\n"
    "v_mfma_f32_16x16x32_bf16 v[156:159], v[148:151], v[224:227], v[156:159]\n"
    "s_waitcnt lgkmcnt(8)\n"
    "v_mfma_f32_16x16x32_bf16 v[164:167], v[148:151], v[228:231], v[164:167]\n"
    "v_mfma_f32_16x16x32_bf16 v[160:163], v[144:147], v[228:231], v[160:163]\n"
    "s_waitcnt lgkmcnt(0)\n"
    "s_waitcnt vmcnt(3)\n"
    "s_barrier\n"
    "v_mfma_f32_16x16x32_bf16 v[168:171], v[232:235], v[144:147], v[168:171]\n"
    "s_add_u32 m0, s55, 106496\n"
    "v_mfma_f32_16x16x32_bf16 v[172:175], v[232:235], v[148:151], v[172:175]\n"
    "buffer_load_dwordx4 v5, s[40:43], s59 offen lds\n"
    "v_mfma_f32_16x16x32_bf16 v[180:183], v[236:239], v[148:151], v[180:183]\n"
    "v_mfma_f32_16x16x32_bf16 v[176:179], v[236:239], v[144:147], v[176:179]\n"
    "v_mfma_f32_16x16x32_bf16 v[184:187], v[240:243], v[144:147], v[184:187]\n"
    "v_mfma_f32_16x16x32_bf16 v[188:191], v[240:243], v[148:151], v[188:191]\n"
    "v_mfma_f32_16x16x32_bf16 v[196:199], v[244:247], v[148:151], v[196:199]\n"
    "v_mfma_f32_16x16x32_bf16 v[192:195], v[244:247], v[144:147], v[192:195]\n"
    "Ljoin_2_%=:\n"
    "s_cmp_eq_u32 s67, 2\n"
    "s_cbranch_scc1 LepiV_3_%=\n"
    "s_cmp_eq_u32 s50, 1.0\n"
    "s_cbranch_scc1 LepiK_7_%=\n"
    "v_pk_mul_f32 v[152:153], v[152:153], s[50:51]\n"
    "v_pk_mul_f32 v[154:155], v[154:155], s[50:51]\n"
    "v_pk_mul_f32 v[156:157], v[156:157], s[50:51]\n"
    "v_pk_mul_f32 v[158:159], v[158:159], s[50:51]\n"
    "v_cvt_pk_bf16_f32 v152, v152, v153\n"
    "v_cvt_pk_bf16_f32 v153, v154, v155\n"
    "v_cvt_pk_bf16_f32 v154, v156, v157\n"
    "v_cvt_pk_bf16_f32 v155, v158, v159\n"
    "s_nop 0\n"
    "s_nop 0\n"
    "v_permlane16_swap_b32 v152, v154\n"
    "v_permlane16_swap_b32 v153, v155\n"
    "buffer_store_dwordx4 v[152:155], v248, s[44:47], s70 offen sc1\n"
    "v_pk_mul_f32 v[160:161], v[160:161], s[50:51]\n"
    "v_pk_mul_f32 v[162:163], v[162:163], s[50:51]\n"
    "v_pk_mul_f32 v[164:165], v[164:165], s[50:51]\n"
    "v_pk_mul_f32 v[166:167], v[166:167], s[50:51]\n"
    "v_cvt_pk_bf16_f32 v160, v160, v161\n"
    "v_cvt_pk_bf16_f32 v161, v162, v163\n"
    "v_cvt_pk_bf16_f32 v162, v164, v165\n"
    "v_cvt_pk_bf16_f32 v163, v166, v167\n"
    "s_add_u32 s70, s70, s71\n"
    "s_nop 0\n"
    "v_permlane16_swap_b32 v160, v162\n"
    "v_permlane16_swap_b32 v161, v163\n"
    "buffer_store_dwordx4 v[160:163], v248, s[44:47], s70 offen sc1\n"
    "s_cmp_eq_u32 s67, 0\n"
    "s_cbranch_scc0 LepiH_5_%=\n"
    "v_pk_mul_f32 v[168:169], v[168:169], s[50:51]\n"
    "v_pk_mul_f32 v[170:171], v[170:171], s[50:51]\n"
    "v_pk_mul_f32 v[172:173], v[172:173], s[50:51]\n"
    "v_pk_mul_f32 v[174:175], v[174:175], s[50:51]\n"
    "v_cvt_pk_bf16_f32 v168, v168, v169\n"
    "v_cvt_pk_bf16_f32 v169, v170, v171\n"
    "v_cvt_pk_bf16_f32 v170, v172, v173\n"
    "v_cvt_pk_bf16_f32 v171, v174, v175\n"
    "s_add_u32 s70, s70, s71\n"
    "s_nop 0\n"
    "v_permlane16_swap_b32 v168, v170\n"
    "v_permlane16_swap_b32 v169, v171\n"
    "buffer_store_dwordx4 v[168:171], v248, s[44:47], s70 offen sc1\n"
    "v_pk_mul_f32 v[176:177], v[176:177], s[50:51]\n"
    "v_pk_mul_f32 v[178:179], v[178:179], s[50:51]\n"
    "v_pk_mul_f32 v[180:181], v[180:181], s[50:51]\n"
    "v_pk_mul_f32 v[182:183], v[182:183], s[50:51]\n"
    "v_cvt_pk_bf16_f32 v176, v176, v177\n"
    "v_cvt_pk_bf16_f32 v177, v178, v179\n"
    "v_cvt_pk_bf16_f32 v178, v180, v181\n"
    "v_cvt_pk_bf16_f32 v179, v182, v183\n"
    "s_add_u32 s70, s70, s71\n"
    "s_nop 0\n"
    "v_permlane16_swap_b32 v176, v178\n"
    "v_permlane16_swap_b32 v177, v179\n"
    "buffer_store_dwordx4 v[176:179], v248, s[44:47], s70 offen sc1\n"
    "v_pk_mul_f32 v[184:185], v[184:185], s[50:51]\n"
    "v_pk_mul_f32 v[186:187], v[186:187], s[50:51]\n"
    "v_pk_mul_f32 v[188:189], v[188:189], s[50:51]\n"
    "v_pk_mul_f32 v[190:191], v[190:191], s[50:51]\n"
    "v_cvt_pk_bf16_f32 v184, v184, v185\n"
    "v_cvt_pk_bf16_f32 v185, v186, v187\n"
    "v_cvt_pk_bf16_f32 v186, v188, v189\n"
    "v_cvt_pk_bf16_f32 v187, v190, v191\n"
    "s_add_u32 s70, s70, s71\n"
    "s_nop 0\n"
    "v_permlane16_swap_b32 v184, v186\n"
    "v_permlane16_swap_b32 v185, v187\n"
    "buffer_store_dwordx4 v[184:187], v248, s[44:47], s70 offen sc1\n"
    "v_pk_mul_f32 v[192:193], v[192:193], s[50:51]\n"
    "v_pk_mul_f32 v[194:195], v[194:195], s[50:51]\n"
    "v_pk_mul_f32 v[196:197], v[196:197], s[50:51]\n"
    "v_pk_mul_f32 v[198:199], v[198:199], s[50:51]\n"
    "v_cvt_pk_bf16_f32 v192, v192, v193\n"
    "v_cvt_pk_bf16_f32 v193, v194, v195\n"
    "v_cvt_pk_bf16_f32 v194, v196, v197\n"
    "v_cvt_pk_bf16_f32 v195, v198, v199\n"
    "s_add_u32 s70, s70, s71\n"
    "s_nop 0\n"
    "v_permlane16_swap_b32 v192, v194\n"
    "v_permlane16_swap_b32 v193, v195\n"
    "buffer_store_dwordx4 v[192:195], v248, s[44:47], s70 offen sc1\n"
    "s_branch LepiJ_6_%=\n"
    "LepiK_7_%=:\n"
    "s_nop 1\n"
    "v_cvt_pk_bf16_f32 v152, v152, v153\n"
    "v_cvt_pk_bf16_f32 v153, v154, v155\n"
    "v_cvt_pk_bf16_f32 v154, v156, v157\n"
    "v_cvt_pk_bf16_f32 v155, v158, v159\n"
    "s_nop 0\n"
    "s_nop 0\n"
    "v_permlane16_swap_b32 v152, v154\n"
    "v_permlane16_swap_b32 v153, v155\n"
    "buffer_store_dwordx4 v[152:155], v248, s[44:47], s70 offen sc1\n"
    "v_cvt_pk_bf16_f32 v160, v160, v161\n"
    "v_cvt_pk_bf16_f32 v161, v162, v163\n"
    "v_cvt_pk_bf16_f32 v162, v164, v165\n"
    "v_cvt_pk_bf16_f32 v163, v166, v167\n"
    "s_add_u32 s70, s70, s71\n"
    "s_nop 0\n"
    "v_permlane16_swap_b32 v160, v162\n"
    "v_permlane16_swap_b32 v161, v163\n"
    "buffer_store_dwordx4 v[160:163], v248, s[44:47], s70 offen sc1\n"
    "s_cmp_eq_u32 s67, 0\n"
    "s_cbranch_scc0 LepiH_5_%=\n"
    "v_cvt_pk_bf16_f32 v168, v168, v169\n"
    "v_cvt_pk_bf16_f32 v169, v170, v171\n"
    "v_cvt_pk_bf16_f32 v170, v172, v173\n"
    "v_cvt_pk_bf16_f32 v171, v174, v175\n"
    "s_add_u32 s70, s70, s71\n"
    "s_nop 0\n"
    "v_permlane16_swap_b32 v168, v170\n"
    "v_permlane16_swap_b32 v169, v171\n"
    "buffer_store_dwordx4 v[168:171], v248, s[44:47], s70 offen sc1\n"
    "v_cvt_pk_bf16_f32 v176, v176, v177\n"
    "v_cvt_pk_bf16_f32 v177, v178, v179\n"
    "v_cvt_pk_bf16_f32 v178, v180, v181\n"
    "v_cvt_pk_bf16_f32 v179, v182, v183\n"
    "s_add_u32 s70, s70, s71\n"
    "s_nop 0\n"
    "v_permlane16_swap_b32 v176, v178\n"
    "v_permlane16_swap_b32 v177, v179\n"
    "buffer_store_dwordx4 v[176:179], v248, s[44:47], s70 offen sc1\n"
    "v_cvt_pk_bf16_f32 v184, v184, v185\n"
    "v_cvt_pk_bf16_f32 v185, v186, v187\n"
    "v_cvt_pk_bf16_f32 v186, v188, v189\n"
    "v_cvt_pk_bf16_f32 v187, v190, v191\n"
    "s_add_u32 s70, s70, s71\n"
    "s_nop 0\n"
    "v_permlane16_swap_b32 v184, v186\n"
    "v_permlane16_swap_b32 v185, v187\n"
    "buffer_store_dwordx4 v[184:187], v248, s[44:47], s70 offen sc1\n"
    "v_cvt_pk_bf16_f32 v192, v192, v193\n"
    "v_cvt_pk_bf16_f32 v193, v194, v195\n"
    "v_cvt_pk_bf16_f32 v194, v196, v197\n"
    "v_cvt_pk_bf16_f32 v195, v198, v199\n"
    "s_add_u32 s70, s70, s71\n"
    "s_nop 0\n"
    "v_permlane16_swap_b32 v192, v194\n"
    "v_permlane16_swap_b32 v193, v195\n"
    "buffer_store_dwordx4 v[192:195], v248, s[44:47], s70 offen sc1\n"
    "s_branch LepiJ_6_%=\n"
    "LepiV_3_%=:\n"
    "v_cvt_pk_bf16_f32 v152, v152, v153\n"
    "v_cvt_pk_bf16_f32 v153, v154, v155\n"
    "v_cvt_pk_bf16_f32 v154, v156, v157\n"
    "v_cvt_pk_bf16_f32 v155, v158, v159\n"
    "buffer_store_dwordx4 v[152:155], v1, s[44:47], s70 offen offset:1024 sc1\n"
    "v_cvt_pk_bf16_f32 v160, v160, v161\n"
    "v_cvt_pk_bf16_f32 v161, v162, v163\n"
    "v_cvt_pk_bf16_f32 v162, v164, v165\n"
    "v_cvt_pk_bf16_f32 v163, v166, v167\n"
    "s_add_u32 s70, s70, s71\n"
    "buffer_store_dwordx4 v[160:163], v1, s[44:47], s70 offen offset:1024 sc1\n"
    "LepiH_5_%=:\n"
    "v_pk_mul_f32 v[168:169], v[168:169], v[176:177]\n"
    "v_pk_mul_f32 v[170:171], v[170:171], v[178:179]\n"
    "v_pk_mul_f32 v[172:173], v[172:173], v[180:181]\n"
    "v_pk_mul_f32 v[174:175], v[174:175], v[182:183]\n"
    "v_pk_mul_f32 v[184:185], v[184:185], v[192:193]\n"
    "v_pk_mul_f32 v[186:187], v[186:187], v[194:195]\n"
    "v_pk_mul_f32 v[188:189], v[188:189], v[196:197]\n"
    "v_pk_mul_f32 v[190:191], v[190:191], v[198:199]\n"
    "v_cvt_pk_bf16_f32 v224, v168, v169\n"
    "v_cvt_pk_bf16_f32 v225, v170, v171\n"
    "v_cvt_pk_bf16_f32 v226, v184, v185\n"
    "v_cvt_pk_bf16_f32 v227, v186, v187\n"
    "v_cvt_pk_bf16_f32 v228, v172, v173\n"
    "v_cvt_pk_bf16_f32 v229, v174, v175\n"
    "v_cvt_pk_bf16_f32 v230, v188, v189\n"
    "v_cvt_pk_bf16_f32 v231, v190, v191\n"
    "LepiJ_6_%=:\n"
    "v_mov_b32 v18, v252\n"
    "v_add_u32 v19, 0x2000, v252\n"
    "v_xor_b32 v20, 32, v252\n"
    "v_add_u32 v21, 0x2000, v20\n"
    "ds_read_b64_tr_b16 v[200:201], v18 offset:0\n"
    "ds_read_b64_tr_b16 v[202:203], v18 offset:512\n"
    "ds_read_b64_tr_b16 v[204:205], v19 offset:0\n"
    "ds_read_b64_tr_b16 v[206:207], v19 offset:512\n"
    "ds_read_b64_tr_b16 v[208:209], v20 offset:0\n"
    "ds_read_b64_tr_b16 v[210:211], v20 offset:512\n"
    "ds_read_b64_tr_b16 v[212:213], v21 offset:0\n"
    "ds_read_b64_tr_b16 v[214:215], v21 offset:512\n"
    "ds_read_b64_tr_b16 v[216:217], v18 offset:4096\n"
    "ds_read_b64_tr_b16 v[218:219], v18 offset:4608\n"
    "ds_read_b64_tr_b16 v[220:221], v19 offset:4096\n"
    "ds_read_b64_tr_b16 v[222:223], v19 offset:4608\n"
    "ds_read_b64_tr_b16 v[192:193], v20 offset:4096\n"
    "ds_read_b64_tr_b16 v[194:195], v20 offset:4608\n"
    "s_waitcnt lgkmcnt(12)\n"
    "v_mfma_f32_16x16x32_bf16 v[152:155], v[200:203], v[24:27], 0\n"
    "ds_read_b64_tr_b16 v[196:197], v21 offset:4096\n"
    "ds_read_b64_tr_b16 v[198:199], v21 offset:4608\n"
    "v_mfma_f32_16x16x32_bf16 v[156:159], v[200:203], v[28:31], 0\n"
    "v_add_u32 v18, 0x4000, v18\n"
    "ds_read_b64_tr_b16 v[184:185], v18 offset:0\n"
    "s_waitcnt lgkmcnt(13)\n"
    "v_mfma_f32_16x16x32_bf16 v[164:167], v[204:207], v[28:31], 0\n"
    "ds_read_b64_tr_b16 v[186:187], v18 offset:512\n"
    "v_add_u32 v19, 0x4000, v19\n"
    "ds_read_b64_tr_b16 v[188:189], v19 offset:0\n"
    "v_mfma_f32_16x16x32_bf16 v[160:163], v[204:207], v[24:27], 0\n"
    "s_waitcnt lgkmcnt(13)\n"
    "v_mfma_f32_16x16x32_bf16 v[168:171], v[208:211], v[24:27], 0\n"
    "ds_read_b64_tr_b16 v[190:191], v19 offset:512\n"
    "v_add_u32 v20, 0x4000, v20\n"
    "ds_read_b64_tr_b16 v[232:233], v20 offset:0\n"
    "v_mfma_f32_16x16x32_bf16 v[172:175], v[208:211], v[28:31], 0\n"
    "s_waitcnt lgkmcnt(13)\n"
    "v_mfma_f32_16x16x32_bf16 v[180:183], v[212:215], v[28:31], 0\n"
    "ds_read_b64_tr_b16 v[234:235], v20 offset:512\n"
    "v_add_u32 v21, 0x4000, v21\n"
    "ds_read_b64_tr_b16 v[236:237], v21 offset:0\n"
    "v_mfma_f32_16x16x32_bf16 v[176:179], v[212:215], v[24:27], 0\n"
    "s_waitcnt lgkmcnt(13)\n"
    "v_mfma_f32_16x16x32_bf16 v[152:155], v[216:219], v[32:35], v[152:155]\n"
    "ds_read_b64_tr_b16 v[238:239], v21 offset:512\n"
    "ds_read_b64_tr_b16 v[200:201], v18 offset:4096\n"
    "v_mfma_f32_16x16x32_bf16 v[156:159], v[216:219], v[36:39], v[156:159]\n"
    "s_waitcnt lgkmcnt(13)\n"
    "v_mfma_f32_16x16x32_bf16 v[164:167], v[220:223], v[36:39], v[164:167]\n"
    "ds_read_b64_tr_b16 v[202:203], v18 offset:4608\n"
    "ds_read_b64_tr_b16 v[204:205], v19 offset:4096\n"
    "v_mfma_f32_16x16x32_bf16 v[160:163], v[220:223], v[32:35], v[160:163]\n"
    "s_waitcnt lgkmcnt(13)\n"
    "v_mfma_f32_16x16x32_bf16 v[168:171], v[192:195], v[32:35], v[168:171]\n"
    "ds_read_b64_tr_b16 v[206:207], v19 offset:4608\n"
    "ds_read_b64_tr_b16 v[208:209], v20 offset:4096\n"
    "v_mfma_f32_16x16x32_bf16 v[172:175], v[192:195], v[36:39], v[172:175]\n"
    "s_waitcnt lgkmcnt(13)\n"
    "v_mfma_f32_16x16x32_bf16 v[180:183], v[196:199], v[36:39], v[180:183]\n"
    "ds_read_b64_tr_b16 v[210:211], v20 offset:4608\n"
    "ds_read_b64_tr_b16 v[212:213], v21 offset:4096\n"
    "v_mfma_f32_16x16x32_bf16 v[176:179], v[196:199], v[32:35], v[176:179]\n"
    "s_waitcnt lgkmcnt(13)\n"
    "v_mfma_f32_16x16x32_bf16 v[152:155], v[184:187], v[40:43], v[152:155]\n"
    "ds_read_b64_tr_b16 v[214:215], v21 offset:4608\n"
    "v_add_u32 v18, 0x6000, v18\n"
    "ds_read_b64_tr_b16 v[216:217], v18 offset:0\n"
    "v_mfma_f32_16x16x32_bf16 v[156:159], v[184:187], v[44:47], v[156:159]\n"
    "s_waitcnt lgkmcnt(13)\n"
    "v_mfma_f32_16x16x32_bf16 v[164:167], v[188:191], v[44:47], v[164:167]\n"
    "ds_read_b64_tr_b16 v[218:219], v18 offset:512\n"
    "v_add_u32 v19, 0x6000, v19\n"
    "ds_read_b64_tr_b16 v[220:221], v19 offset:0\n"
    "v_mfma_f32_16x16x32_bf16 v[160:163], v[188:191], v[40:43], v[160:163]\n"
    "s_waitcnt lgkmcnt(13)\n"
    "v_mfma_f32_16x16x32_bf16 v[168:171], v[232:235], v[40:43], v[168:171]\n"
    "ds_read_b64_tr_b16 v[222:223], v19 offset:512\n"
    "v_add_u32 v20, 0x6000, v20\n"
    "ds_read_b64_tr_b16 v[192:193], v20 offset:0\n"
    "v_mfma_f32_16x16x32_bf16 v[172:175], v[232:235], v[44:47], v[172:175]\n"
    "s_waitcnt lgkmcnt(13)\n"
    "v_mfma_f32_16x16x32_bf16 v[180:183], v[236:239], v[44:47], v[180:183]\n"
    "ds_read_b64_tr_b16 v[194:195], v20 offset:512\n"
    "v_add_u32 v21, 0x6000, v21\n"
    "ds_read_b64_tr_b16 v[196:197], v21 offset:0\n"
    "v_mfma_f32_16x16x32_bf16 v[176:179], v[236:239], v[40:43], v[176:179]\n"
    "s_waitcnt lgkmcnt(13)\n"
    "v_mfma_f32_16x16x32_bf16 v[152:155], v[200:203], v[48:51], v[152:155]\n"
    "ds_read_b64_tr_b16 v[198:199], v21 offset:512\n"
    "v_mfma_f32_16x16x32_bf16 v[156:159], v[200:203], v[52:55], v[156:159]\n"
    "s_waitcnt vmcnt(0)\n"
    "s_barrier\n"
    "s_cmp_lg_u32 s52, 0\n"
    "s_cbranch_scc1 Lnof_8_%=\n"
    "v_mov_b64 v[10:11], s[22:23]\n"
    "v_mov_b64 v[12:13], s[22:23]\n"
    "s_mov_b64 exec, 0xff\n"
    "buffer_store_dwordx4 v[10:13], v1, s[44:47], s73 offen sc1\n"
    "s_mov_b64 exec, -1\n"
    "Lnof_8_%=:\n"
    "s_waitcnt lgkmcnt(12)\n"
    "v_mfma_f32_16x16x32_bf16 v[164:167], v[204:207], v[52:55], v[164:167]\n"
    "ds_read_b64_tr_b16 v[184:185], v18 offset:4096\n"
    "ds_read_b64_tr_b16 v[186:187], v18 offset:4608\n"
    "v_mfma_f32_16x16x32_bf16 v[160:163], v[204:207], v[48:51], v[160:163]\n"
    "ds_read_b64_tr_b16 v[188:189], v19 offset:4096\n"
    "s_waitcnt lgkmcnt(13)\n"
    "v_mfma_f32_16x16x32_bf16 v[168:171], v[208:211], v[48:51], v[168:171]\n"
    "ds_read_b64_tr_b16 v[190:191], v19 offset:4608\n"
    "ds_read_b64_tr_b16 v[232:233], v20 offset:4096\n"
    "s_add_u32 m0, s55, 122880\n"
    "v_mfma_f32_16x16x32_bf16 v[172:175], v[208:211], v[52:55], v[172:175]\n"
    "s_mul_i32 s59, s54, 229376\n"
    "s_add_u32 s54, s54, 1\n"
    "s_and_b32 s54, s54, 7\n"
    "buffer_load_dwordx4 v5, s[36:39], s59 offen lds\n"
    "s_waitcnt lgkmcnt(13)\n"
    "v_mfma_f32_16x16x32_bf16 v[180:183], v[212:215], v[52:55], v[180:183]\n"
    "ds_read_b64_tr_b16 v[234:235], v20 offset:4608\n"
    "ds_read_b64_tr_b16 v[236:237], v21 offset:4096\n"
    "s_add_u32 m0, s55, 131072\n"
    "v_mfma_f32_16x16x32_bf16 v[176:179], v[212:215], v[48:51], v[176:179]\n"
    "buffer_load_dwordx4 v5, s[40:43], s59 offen lds\n"
    "s_waitcnt lgkmcnt(13)\n"
    "v_mfma_f32_16x16x32_bf16 v[152:155], v[216:219], v[56:59], v[152:155]\n"
    "ds_read_b64_tr_b16 v[238:239], v21 offset:4608\n"
    "v_add_u32 v18, 0x4000, v18\n"
    "ds_read_b64_tr_b16 v[200:201], v18 offset:0\n"
    "v_mfma_f32_16x16x32_bf16 v[156:159], v[216:219], v[60:63], v[156:159]\n"
    "s_waitcnt lgkmcnt(13)\n"
    "v_mfma_f32_16x16x32_bf16 v[164:167], v[220:223], v[60:63], v[164:167]\n"
    "ds_read_b64_tr_b16 v[202:203], v18 offset:512\n"
    "v_add_u32 v19, 0x4000, v19\n"
    "ds_read_b64_tr_b16 v[204:205], v19 offset:0\n"
    "v_mfma_f32_16x16x32_bf16 v[160:163], v[220:223], v[56:59], v[160:163]\n"
    "s_waitcnt lgkmcnt(13)\n"
    "v_mfma_f32_16x16x32_bf16 v[168:171], v[192:195], v[56:59], v[168:171]\n"
    "ds_read_b64_tr_b16 v[206:207], v19 offset:512\n"
    "v_add_u32 v20, 0x4000, v20\n"
    "ds_read_b64_tr_b16 v[208:209], v20 offset:0\n"
    "s_add_u32 m0, s55, 139264\n"
    "v_mfma_f32_16x16x32_bf16 v[172:175], v[192:195], v[60:63], v[172:175]\n"
    "s_mul_i32 s59, s54, 229376\n"
    "s_add_u32 s54, s54, 1\n"
    "s_and_b32 s54, s54, 7\n"
    "buffer_load_dwordx4 v5, s[36:39], s59 offen lds\n"
    "s_waitcnt lgkmcnt(13)\n"
    "v_mfma_f32_16x16x32_bf16 v[180:183], v[196:199], v[60:63], v[180:183]\n"
    "ds_read_b64_tr_b16 v[210:211], v20 offset:512\n"
    "v_add_u32 v21, 0x4000, v21\n"
    "ds_read_b64_tr_b16 v[212:213], v21 offset:0\n"
    "s_add_u32 m0, s55, 147456\n"
    "v_mfma_f32_16x16x32_bf16 v[176:179], v[196:199], v[56:59], v[176:179]\n"
    "buffer_load_dwordx4 v5, s[40:43], s59 offen lds\n"
    "s_waitcnt lgkmcnt(13)\n"
    "v_mfma_f32_16x16x32_bf16 v[152:155], v[184:187], v[64:67], v[152:155]\n"
    "ds_read_b64_tr_b16 v[214:215], v21 offset:512\n"
    "ds_read_b64_tr_b16 v[216:217], v18 offset:4096\n"
    "v_mfma_f32_16x16x32_bf16 v[156:159], v[184:187], v[68:71], v[156:159]\n"
    "s_waitcnt lgkmcnt(13)\n"
    "v_mfma_f32_16x16x32_bf16 v[164:167], v[188:191], v[68:71], v[164:167]\n"
    "ds_read_b64_tr_b16 v[218:219], v18 offset:4608\n"
    "ds_read_b64_tr_b16 v[220:221], v19 offset:4096\n"
    "v_mfma_f32_16x16x32_bf16 v[160:163], v[188:191], v[64:67], v[160:163]\n"
    "v_mov_b32 v14, v249\n"
    "v_mov_b32 v3, v250\n"
    "v_mov_b32 v5, v251\n"
    "v_mov_b32 v16, v253\n"
    "v_mov_b32 v17, v254\n"
    "v_mov_b32 v22, v255\n"
    "v_mov_b32 v4, 0xdddddddd\n"
    "v_add_u32 v2, 0x1000, v1\n"
    "v_and_b32 v23, 31, v7\n"
    "v_lshlrev_b32 v23, 2, v23\n"
    "s_mov_b64 s[24:25], s[14:15]\n"
    "s_mov_b32 s26, 1835008\n"
    "s_mov_b32 s27, 0x20000\n"
    "s_mov_b64 s[28:29], s[8:9]\n"
    "s_mov_b32 s30, 524288\n"
    "s_mov_b32 s31, 0x20000\n"
    "s_mov_b64 s[36:37], s[18:19]\n"
    "s_mov_b32 s38, 2097152\n"
    "s_mov_b32 s39, 0x20000\n"
    "s_waitcnt lgkmcnt(13)\n"
    "v_mfma_f32_16x16x32_bf16 v[168:171], v[232:235], v[64:67], v[168:171]\n"
    "ds_read_b64_tr_b16 v[222:223], v19 offset:4608\n"
    "ds_read_b64_tr_b16 v[192:193], v20 offset:4096\n"
    "v_mfma_f32_16x16x32_bf16 v[172:175], v[232:235], v[68:71], v[172:175]\n"
    "s_waitcnt lgkmcnt(13)\n"
    "v_mfma_f32_16x16x32_bf16 v[180:183], v[236:239], v[68:71], v[180:183]\n"
    "ds_read_b64_tr_b16 v[194:195], v20 offset:4608\n"
    "ds_read_b64_tr_b16 v[196:197], v21 offset:4096\n"
    "v_mfma_f32_16x16x32_bf16 v[176:179], v[236:239], v[64:67], v[176:179]\n"
    "s_waitcnt lgkmcnt(13)\n"
    "v_mfma_f32_16x16x32_bf16 v[152:155], v[200:203], v[72:75], v[152:155]\n"
    "ds_read_b64_tr_b16 v[198:199], v21 offset:4608\n"
    "v_add_u32 v18, 0x6000, v18\n"
    "ds_read_b64_tr_b16 v[184:185], v18 offset:0\n"
    "v_mfma_f32_16x16x32_bf16 v[156:159], v[200:203], v[76:79], v[156:159]\n"
    "s_waitcnt lgkmcnt(13)\n"
    "v_mfma_f32_16x16x32_bf16 v[164:167], v[204:207], v[76:79], v[164:167]\n"
    "ds_read_b64_tr_b16 v[186:187], v18 offset:512\n"
    "v_add_u32 v19, 0x6000, v19\n"
    "ds_read_b64_tr_b16 v[188:189], v19 offset:0\n"
    "v_mfma_f32_16x16x32_bf16 v[160:163], v[204:207], v[72:75], v[160:163]\n"
    "s_cmp_eq_u32 s67, 0\n"
    "s_cbranch_scc1 Lsk_9_%=\n"
    "buffer_store_dwordx4 v[224:227], v1, s[44:47], s72 offen sc1\n"
    "Lsk_9_%=:\n"
    "s_waitcnt lgkmcnt(13)\n"
    "v_mfma_f32_16x16x32_bf16 v[168:171], v[208:211], v[72:75], v[168:171]\n"
    "ds_read_b64_tr_b16 v[190:191], v19 offset:512\n"
    "v_add_u32 v20, 0x6000, v20\n"
    "ds_read_b64_tr_b16 v[232:233], v20 offset:0\n"
    "v_mfma_f32_16x16x32_bf16 v[172:175], v[208:211], v[76:79], v[172:175]\n"
    "s_waitcnt lgkmcnt(13)\n"
    "v_mfma_f32_16x16x32_bf16 v[180:183], v[212:215], v[76:79], v[180:183]\n"
    "ds_read_b64_tr_b16 v[234:235], v20 offset:512\n"
    "v_add_u32 v21, 0x6000, v21\n"
    "ds_read_b64_tr_b16 v[236:237], v21 offset:0\n"
    "v_mfma_f32_16x16x32_bf16 v[176:179], v[212:215], v[72:75], v[176:179]\n"
    "s_cmp_eq_u32 s67, 0\n"
    "s_cbranch_scc1 Lsk_10_%=\n"
    "s_add_u32 s72, s72, 57344\n"
    "buffer_store_dwordx4 v[228:231], v1, s[44:47], s72 offen sc1\n"
    "Lsk_10_%=:\n"
    "s_waitcnt lgkmcnt(13)\n"
    "v_mfma_f32_16x16x32_bf16 v[152:155], v[216:219], v[80:83], v[152:155]\n"
    "ds_read_b64_tr_b16 v[238:239], v21 offset:512\n"
    "ds_read_b64_tr_b16 v[200:201], v18 offset:4096\n"
    "v_mfma_f32_16x16x32_bf16 v[156:159], v[216:219], v[84:87], v[156:159]\n"
    "s_waitcnt lgkmcnt(13)\n"
    "v_mfma_f32_16x16x32_bf16 v[164:167], v[220:223], v[84:87], v[164:167]\n"
    "ds_read_b64_tr_b16 v[202:203], v18 offset:4608\n"
    "ds_read_b64_tr_b16 v[204:205], v19 offset:4096\n"
    "v_mfma_f32_16x16x32_bf16 v[160:163], v[220:223], v[80:83], v[160:163]\n"
    "s_waitcnt lgkmcnt(13)\n"
    "v_mfma_f32_16x16x32_bf16 v[168:171], v[192:195], v[80:83], v[168:171]\n"
    "ds_read_b64_tr_b16 v[206:207], v19 offset:4608\n"
    "ds_read_b64_tr_b16 v[208:209], v20 offset:4096\n"
    "v_mfma_f32_16x16x32_bf16 v[172:175], v[192:195], v[84:87], v[172:175]\n"
    "s_waitcnt lgkmcnt(13)\n"
    "v_mfma_f32_16x16x32_bf16 v[180:183], v[196:199], v[84:87], v[180:183]\n"
    "ds_read_b64_tr_b16 v[210:211], v20 offset:4608\n"
    "ds_read_b64_tr_b16 v[212:213], v21 offset:4096\n"
    "v_mfma_f32_16x16x32_bf16 v[176:179], v[196:199], v[80:83], v[176:179]\n"
    "s_waitcnt lgkmcnt(13)\n"
    "v_mfma_f32_16x16x32_bf16 v[152:155], v[184:187], v[88:91], v[152:155]\n"
    "ds_read_b64_tr_b16 v[214:215], v21 offset:4608\n"
    "v_add_u32 v18, 0x4000, v18\n"
    "ds_read_b64_tr_b16 v[216:217], v18 offset:0\n"
    "v_mfma_f32_16x16x32_bf16 v[156:159], v[184:187], v[92:95], v[156:159]\n"
    "s_waitcnt lgkmcnt(13)\n"
    "v_mfma_f32_16x16x32_bf16 v[164:167], v[188:191], v[92:95], v[164:167]\n"
    "ds_read_b64_tr_b16 v[218:219], v18 offset:512\n"
    "v_add_u32 v19, 0x4000, v19\n"
    "ds_read_b64_tr_b16 v[220:221], v19 offset:0\n"
    "v_mfma_f32_16x16x32_bf16 v[160:163], v[188:191], v[88:91], v[160:163]\n"
    "s_waitcnt lgkmcnt(13)\n"
    "v_mfma_f32_16x16x32_bf16 v[168:171], v[232:235], v[88:91], v[168:171]\n"
    "ds_read_b64_tr_b16 v[222:223], v19 offset:512\n"
    "v_add_u32 v20, 0x4000, v20\n"
    "ds_read_b64_tr_b16 v[192:193], v20 offset:0\n"
    "v_mfma_f32_16x16x32_bf16 v[172:175], v[232:235], v[92:95], v[172:175]\n"
    "s_waitcnt lgkmcnt(13)\n"
    "v_mfma_f32_16x16x32_bf16 v[180:183], v[236:239], v[92:95], v[180:183]\n"
    "ds_read_b64_tr_b16 v[194:195], v20 offset:512\n"
    "v_add_u32 v21, 0x4000, v21\n"
    "ds_read_b64_tr_b16 v[196:197], v21 offset:0\n"
    "v_mfma_f32_16x16x32_bf16 v[176:179], v[236:239], v[88:91], v[176:179]\n"
    "s_waitcnt lgkmcnt(13)\n"
    "v_mfma_f32_16x16x32_bf16 v[152:155], v[200:203], v[96:99], v[152:155]\n"
    "ds_read_b64_tr_b16 v[198:199], v21 offset:512\n"
    "ds_read_b64_tr_b16 v[56:57], v18 offset:4096\n"
    "v_mfma_f32_16x16x32_bf16 v[156:159], v[200:203], v[100:103], v[156:159]\n"
    "s_waitcnt lgkmcnt(13)\n"
    "v_mfma_f32_16x16x32_bf16 v[164:167], v[204:207], v[100:103], v[164:167]\n"
    "ds_read_b64_tr_b16 v[58:59], v18 offset:4608\n"
    "ds_read_b64_tr_b16 v[60:61], v19 offset:4096\n"
    "v_mfma_f32_16x16x32_bf16 v[160:163], v[204:207], v[96:99], v[160:163]\n"
    "s_waitcnt lgkmcnt(13)\n"
    "v_mfma_f32_16x16x32_bf16 v[168:171], v[208:211], v[96:99], v[168:171]\n"
    "ds_read_b64_tr_b16 v[62:63], v19 offset:4608\n"
    "ds_read_b64_tr_b16 v[64:65], v20 offset:4096\n"
    "v_mfma_f32_16x16x32_bf16 v[172:175], v[208:211], v[100:103], v[172:175]\n"
    "v_mov_b64 v[248:249], 0\n"
    "s_mov_b32 exec_lo, 0x00110000\n"
    "s_mov_b32 exec_hi, 0x00110000\n"
    "v_mov_b32 v248, 0x3f803f80\n"
    "v_mov_b32 v249, 0x3f803f80\n"
    "s_mov_b64 exec, -1\n"
    "v_mov_b64 v[250:251], v[248:249]\n"
    "v_mov_b64 v[252:253], v[248:249]\n"
    "v_mov_b64 v[254:255], v[248:249]\n"
    "v_mov_b64 v[236:237], v[248:249]\n"
    "v_mov_b64 v[238:239], v[248:249]\n"
    "v_mov_b64 v[240:241], v[248:249]\n"
    "v_mov_b64 v[242:243], v[248:249]\n"
    "s_waitcnt lgkmcnt(13)\n"
    "v_mfma_f32_16x16x32_bf16 v[180:183], v[212:215], v[100:103], v[180:183]\n"
    "ds_read_b64_tr_b16 v[66:67], v20 offset:4608\n"
    "ds_read_b64_tr_b16 v[68:69], v21 offset:4096\n"
    "v_mfma_f32_16x16x32_bf16 v[176:179], v[212:215], v[96:99], v[176:179]\n"
    "s_waitcnt lgkmcnt(13)\n"
    "v_mfma_f32_16x16x32_bf16 v[152:155], v[216:219], v[104:107], v[152:155]\n"
    "ds_read_b64_tr_b16 v[70:71], v21 offset:4608\n"
    "v_mfma_f32_16x16x32_bf16 v[156:159], v[216:219], v[108:111], v[156:159]\n"
    "s_waitcnt vmcnt(0)\n"
    "s_barrier\n"
    "s_waitcnt lgkmcnt(12)\n"
    "v_mfma_f32_16x16x32_bf16 v[164:167], v[220:223], v[108:111], v[164:167]\n"
    "v_add_u32 v18, 0x6000, v18\n"
    "ds_read_b64_tr_b16 v[200:201], v18 offset:0\n"
    "ds_read_b64_tr_b16 v[202:203], v18 offset:512\n"
    "v_mfma_f32_16x16x32_bf16 v[160:163], v[220:223], v[104:107], v[160:163]\n"
    "v_add_u32 v19, 0x6000, v19\n"
    "ds_read_b64_tr_b16 v[204:205], v19 offset:0\n"
    "s_waitcnt lgkmcnt(13)\n"
    "v_mfma_f32_16x16x32_bf16 v[168:171], v[192:195], v[104:107], v[168:171]\n"
    "ds_read_b64_tr_b16 v[206:207], v19 offset:512\n"
    "v_add_u32 v20, 0x6000, v20\n"
    "ds_read_b64_tr_b16 v[208:209], v20 offset:0\n"
    "v_mfma_f32_16x16x32_bf16 v[172:175], v[192:195], v[108:111], v[172:175]\n"
    "s_waitcnt lgkmcnt(13)\n"
    "v_mfma_f32_16x16x32_bf16 v[180:183], v[196:199], v[108:111], v[180:183]\n"
    "ds_read_b64_tr_b16 v[210:211], v20 offset:512\n"
    "v_add_u32 v21, 0x6000, v21\n"
    "ds_read_b64_tr_b16 v[212:213], v21 offset:0\n"
    "v_mfma_f32_16x16x32_bf16 v[176:179], v[196:199], v[104:107], v[176:179]\n"
    "buffer_load_dwordx2 v[8:9], v14, s[44:47], 0 offen sc1\n"
    "s_waitcnt lgkmcnt(13)\n"
    "v_mfma_f32_16x16x32_bf16 v[152:155], v[56:59], v[112:115], v[152:155]\n"
    "ds_read_b64_tr_b16 v[214:215], v21 offset:512\n"
    "ds_read_b64_tr_b16 v[216:217], v18 offset:4096\n"
    "v_mfma_f32_16x16x32_bf16 v[156:159], v[56:59], v[116:119], v[156:159]\n"
    "s_waitcnt lgkmcnt(13)\n"
    "v_mfma_f32_16x16x32_bf16 v[164:167], v[60:63], v[116:119], v[164:167]\n"
    "ds_read_b64_tr_b16 v[218:219], v18 offset:4608\n"
    "ds_read_b64_tr_b16 v[220:221], v19 offset:4096\n"
    "v_mfma_f32_16x16x32_bf16 v[160:163], v[60:63], v[112:115], v[160:163]\n"
    "s_waitcnt lgkmcnt(13)\n"
    "v_mfma_f32_16x16x32_bf16 v[168:171], v[64:67], v[112:115], v[168:171]\n"
    "ds_read_b64_tr_b16 v[222:223], v19 offset:4608\n"
    "ds_read_b64_tr_b16 v[192:193], v20 offset:4096\n"
    "v_mfma_f32_16x16x32_bf16 v[172:175], v[64:67], v[116:119], v[172:175]\n"
    "s_waitcnt lgkmcnt(13)\n"
    "v_mfma_f32_16x16x32_bf16 v[180:183], v[68:71], v[116:119], v[180:183]\n"
    "ds_read_b64_tr_b16 v[194:195], v20 offset:4608\n"
    "ds_read_b64_tr_b16 v[196:197], v21 offset:4096\n"
    "v_mfma_f32_16x16x32_bf16 v[176:179], v[68:71], v[112:115], v[176:179]\n"
    "s_waitcnt lgkmcnt(13)\n"
    "v_mfma_f32_16x16x32_bf16 v[152:155], v[200:203], v[120:123], v[152:155]\n"
    "ds_read_b64_tr_b16 v[198:199], v21 offset:4608\n"
    "v_add_u32 v18, 0x4000, v18\n"
    "ds_read_b64_tr_b16 v[56:57], v18 offset:0\n"
    "v_mfma_f32_16x16x32_bf16 v[156:159], v[200:203], v[124:127], v[156:159]\n"
    "s_waitcnt lgkmcnt(13)\n"
    "v_mfma_f32_16x16x32_bf16 v[164:167], v[204:207], v[124:127], v[164:167]\n"
    "ds_read_b64_tr_b16 v[58:59], v18 offset:512\n"
    "v_add_u32 v19, 0x4000, v19\n"
    "ds_read_b64_tr_b16 v[60:61], v19 offset:0\n"
    "v_mfma_f32_16x16x32_bf16 v[160:163], v[204:207], v[120:123], v[160:163]\n"
    "s_waitcnt lgkmcnt(13)\n"
    "v_mfma_f32_16x16x32_bf16 v[168:171], v[208:211], v[120:123], v[168:171]\n"
    "ds_read_b64_tr_b16 v[62:63], v19 offset:512\n"
    "v_add_u32 v20, 0x4000, v20\n"
    "ds_read_b64_tr_b16 v[64:65], v20 offset:0\n"
    "v_mfma_f32_16x16x32_bf16 v[172:175], v[208:211], v[124:127], v[172:175]\n"
    "s_waitcnt lgkmcnt(13)\n"
    "v_mfma_f32_16x16x32_bf16 v[180:183], v[212:215], v[124:127], v[180:183]\n"
    "ds_read_b64_tr_b16 v[66:67], v20 offset:512\n"
    "v_add_u32 v21, 0x4000, v21\n"
    "ds_read_b64_tr_b16 v[68:69], v21 offset:0\n"
    "v_mfma_f32_16x16x32_bf16 v[176:179], v[212:215], v[120:123], v[176:179]\n"
    "s_waitcnt lgkmcnt(13)\n"
    "v_mfma_f32_16x16x32_bf16 v[152:155], v[216:219], v[128:131], v[152:155]\n"
    "ds_read_b64_tr_b16 v[70:71], v21 offset:512\n"
    "ds_read_b64_tr_b16 v[200:201], v18 offset:4096\n"
    "v_mfma_f32_16x16x32_bf16 v[156:159], v[216:219], v[132:135], v[156:159]\n"
    "s_waitcnt vmcnt(0)\n"
    "v_cmp_ne_u64 vcc, s[22:23], v[8:9]\n"
    "s_cmp_lg_u64 vcc, 0\n"
    "s_cselect_b32 s68, 1, 0\n"
    "s_waitcnt lgkmcnt(13)\n"
    "v_mfma_f32_16x16x32_bf16 v[164:167], v[220:223], v[132:135], v[164:167]\n"
    "ds_read_b64_tr_b16 v[202:203], v18 offset:4608\n"
    "ds_read_b64_tr_b16 v[204:205], v19 offset:4096\n"
    "v_mfma_f32_16x16x32_bf16 v[160:163], v[220:223], v[128:131], v[160:163]\n"
    "s_cmp_lg_u32 s68, 0\n"
    "s_cbranch_scc1 Lsk_11_%=\n"
    "buffer_load_dwordx4 v[40:43], v1, s[44:47], s76 offen sc1\n"
    "buffer_load_dwordx4 v[24:27], v1, s[44:47], s75 offen offset:0 sc1\n"
    "buffer_load_dwordx4 v[28:31], v1, s[44:47], s75 offen offset:1024 sc1\n"
    "Lsk_11_%=:\n"
    "s_waitcnt lgkmcnt(13)\n"
    "v_mfma_f32_16x16x32_bf16 v[168:171], v[192:195], v[128:131], v[168:171]\n"
    "ds_read_b64_tr_b16 v[206:207], v19 offset:4608\n"
    "ds_read_b64_tr_b16 v[208:209], v20 offset:4096\n"
    "v_mfma_f32_16x16x32_bf16 v[172:175], v[192:195], v[132:135], v[172:175]\n"
    "s_waitcnt lgkmcnt(13)\n"
    "v_mfma_f32_16x16x32_bf16 v[180:183], v[196:199], v[132:135], v[180:183]\n"
    "ds_read_b64_tr_b16 v[210:211], v20 offset:4608\n"
    "ds_read_b64_tr_b16 v[212:213], v21 offset:4096\n"
    "v_mfma_f32_16x16x32_bf16 v[176:179], v[196:199], v[128:131], v[176:179]\n"
    "s_cmp_lg_u32 s68, 0\n"
    "s_cbranch_scc1 Lsk_12_%=\n"
    "buffer_load_dwordx4 v[32:35], v1, s[44:47], s75 offen offset:2048 sc1\n"
    "buffer_load_dwordx4 v[36:39], v1, s[44:47], s75 offen offset:3072 sc1\n"
    "s_add_u32 s79, s77, s78\n"
    "s_add_u32 s78, s78, 4096\n"
    "buffer_load_dwordx4 v[244:247], v1, s[44:47], s79 offen offset:0 sc1\n"
    "s_and_b32 s78, s78, 32767\n"
    "Lsk_12_%=:\n"
    "s_waitcnt lgkmcnt(13)\n"
    "v_mfma_f32_16x16x32_bf16 v[152:155], v[56:59], v[136:139], v[152:155]\n"
    "ds_read_b64_tr_b16 v[214:215], v21 offset:4608\n"
    "v_mfma_f32_16x16x32_bf16 v[156:159], v[56:59], v[140:143], v[156:159]\n"
    "s_waitcnt lgkmcnt(12)\n"
    "v_mfma_f32_16x16x32_bf16 v[164:167], v[60:63], v[140:143], v[164:167]\n"
    "v_mfma_f32_16x16x32_bf16 v[160:163], v[60:63], v[136:139], v[160:163]\n"
    "s_waitcnt lgkmcnt(10)\n"
    "v_mfma_f32_16x16x32_bf16 v[168:171], v[64:67], v[136:139], v[168:171]\n"
    "v_mfma_f32_16x16x32_bf16 v[172:175], v[64:67], v[140:143], v[172:175]\n"
    "s_waitcnt lgkmcnt(8)\n"
    "v_mfma_f32_16x16x32_bf16 v[180:183], v[68:71], v[140:143], v[180:183]\n"
    "v_mfma_f32_16x16x32_bf16 v[176:179], v[68:71], v[136:139], v[176:179]\n"
    "s_waitcnt lgkmcnt(6)\n"
    "v_mfma_f32_16x16x32_bf16 v[152:155], v[200:203], v[144:147], v[152:155]\n"
    "v_mfma_f32_16x16x32_bf16 v[156:159], v[200:203], v[148:151], v[156:159]\n"
    "s_waitcnt lgkmcnt(0)\n"
    "s_barrier\n"
    "v_mfma_f32_16x16x32_bf16 v[164:167], v[204:207], v[148:151], v[164:167]\n"
    "v_mfma_f32_16x16x32_bf16 v[160:163], v[204:207], v[144:147], v[160:163]\n"
    "v_mfma_f32_16x16x32_bf16 v[168:171], v[208:211], v[144:147], v[168:171]\n"
    "v_mfma_f32_16x16x32_bf16 v[172:175], v[208:211], v[148:151], v[172:175]\n"
    "v_mfma_f32_16x16x32_bf16 v[180:183], v[212:215], v[148:151], v[180:183]\n"
    "v_mfma_f32_16x16x32_bf16 v[176:179], v[212:215], v[144:147], v[176:179]\n"
    "s_cmp_lg_u32 s68, 0\n"
    "s_cbranch_scc1 Lsk_13_%=\n"
    "s_mov_b64 exec, s[34:35]\n"
    "buffer_load_dwordx4 v[248:251], v3, s[44:47], s79 offen offset:1024 sc1\n"
    "buffer_load_dwordx4 v[252:255], v3, s[44:47], s79 offen offset:1536 sc1\n"
    "s_mov_b64 exec, -1\n"
    "buffer_load_dwordx4 v[232:235], v1, s[44:47], s79 offen offset:2048 sc1\n"
    "buffer_load_dword v23, v23, s[44:47], s21 offen sc1\n"
    "Lsk_13_%=:\n"
    "s_cmp_eq_u32 s68, 0\n"
    "s_cbranch_scc1 Lgo_14_%=\n"
    "s_mov_b32 s66, 65536\n"
    "Lpoll_15_%=:\n"
    "buffer_load_dwordx2 v[8:9], v14, s[44:47], 0 offen sc1\n"
    "s_waitcnt vmcnt(0)\n"
    "Lchk_17_%=:\n"
    "v_cmp_ne_u64 vcc, s[22:23], v[8:9]\n"
    "s_cbranch_vccz Ldone_16_%=\n"
    "s_sleep 3\n"
    "s_sub_u32 s66, s66, 1\n"
    "s_cmp_lg_u32 s66, 0\n"
    "s_cbranch_scc1 Lpoll_15_%=\n"
    "Ldone_16_%=:\n"
    "buffer_load_dwordx4 v[40:43], v1, s[44:47], s76 offen sc1\n"
    "buffer_load_dwordx4 v[24:27], v1, s[44:47], s75 offen offset:0 sc1\n"
    "buffer_load_dwordx4 v[28:31], v1, s[44:47], s75 offen offset:1024 sc1\n"
    "buffer_load_dwordx4 v[32:35], v1, s[44:47], s75 offen offset:2048 sc1\n"
    "buffer_load_dwordx4 v[36:39], v1, s[44:47], s75 offen offset:3072 sc1\n"
    "s_add_u32 s79, s77, s78\n"
    "s_add_u32 s78, s78, 4096\n"
    "buffer_load_dwordx4 v[244:247], v1, s[44:47], s79 offen offset:0 sc1\n"
    "s_and_b32 s78, s78, 32767\n"
    "s_mov_b64 exec, s[34:35]\n"
    "buffer_load_dwordx4 v[248:251], v3, s[44:47], s79 offen offset:1024 sc1\n"
    "buffer_load_dwordx4 v[252:255], v3, s[44:47], s79 offen offset:1536 sc1\n"
    "s_mov_b64 exec, -1\n"
    "buffer_load_dwordx4 v[232:235], v1, s[44:47], s79 offen offset:2048 sc1\n"
    "buffer_load_dword v23, v23, s[44:47], s21 offen sc1\n"
    "Lgo_14_%=:\n"
    "v_mov_b64 v[192:193], 0\n"
    "v_mov_b64 v[194:195], 0\n"
    "s_waitcnt vmcnt(5)\n"
    "v_mfma_f32_32x32x16_bf16 v[56:71], v[40:43], v[24:27], 0\n"
    "v_mfma_f32_32x32x16_bf16 v[72:87], v[40:43], v[28:31], 0\n"
    "v_mfma_f32_32x32x16_bf16 v[200:215], v[40:43], v[32:35], 0\n"
    "v_mfma_f32_32x32x16_bf16 v[216:231], v[40:43], v[36:39], 0\n"
    "v_pk_mul_f32 v[152:153], v[152:153], v[160:161]\n"
    "v_pk_mul_f32 v[154:155], v[154:155], v[162:163]\n"
    "v_pk_mul_f32 v[156:157], v[156:157], v[164:165]\n"
    "v_pk_mul_f32 v[158:159], v[158:159], v[166:167]\n"
    "v_pk_mul_f32 v[168:169], v[168:169], v[176:177]\n"
    "v_pk_mul_f32 v[170:171], v[170:171], v[178:179]\n"
    "v_pk_mul_f32 v[172:173], v[172:173], v[180:181]\n"
    "v_pk_mul_f32 v[174:175], v[174:175], v[182:183]\n"
    "v_cvt_pk_bf16_f32 v184, v152, v153\n"
    "v_cvt_pk_bf16_f32 v185, v154, v155\n"
    "v_cvt_pk_bf16_f32 v186, v168, v169\n"
    "v_cvt_pk_bf16_f32 v187, v170, v171\n"
    "v_cvt_pk_bf16_f32 v188, v156, v157\n"
    "v_cvt_pk_bf16_f32 v189, v158, v159\n"
    "v_cvt_pk_bf16_f32 v190, v172, v173\n"
    "v_cvt_pk_bf16_f32 v191, v174, v175\n"
    "v_max3_f32 v13, v56, v57, v56\n"
    "v_max3_f32 v13, v58, v59, v13\n"
    "v_max3_f32 v13, v72, v73, v13\n"
    "v_max3_f32 v13, v74, v75, v13\n"
    "v_max3_f32 v13, v200, v201, v13\n"
    "v_max3_f32 v13, v202, v203, v13\n"
    "v_max3_f32 v13, v216, v217, v13\n"
    "v_max3_f32 v13, v218, v219, v13\n"
    "v_mov_b32 v10, v13\n"
    "v_add_u32 v14, s83, v1\n"
    "s_nop 0\n"
    "v_permlane32_swap_b32 v10, v13\n"
    "s_nop 1\n"
    "v_max_f32 v13, v13, v10\n"
    "v_sub_f32 v40, 0, v13\n"
    "v_sub_f32 v41, 0, v13\n"
    "v_mov_b64 v[42:43], v[40:41]\n"
    "v_mov_b64 v[44:45], v[40:41]\n"
    "v_mov_b64 v[46:47], v[40:41]\n"
    "v_mov_b64 v[48:49], v[40:41]\n"
    "v_mov_b64 v[50:51], v[40:41]\n"
    "v_mov_b64 v[52:53], v[40:41]\n"
    "v_mov_b64 v[54:55], v[40:41]\n"
    "s_waitcnt vmcnt(4)\n"
    "s_nop 0\n"
    "v_mfma_f32_32x32x16_bf16 v[56:71], v[244:247], v[24:27], v[40:55]\n"
    "v_mfma_f32_32x32x16_bf16 v[88:103], v[192:195], v[192:195], 0\n"
    "v_mov_b32 v18, v16\n"
    "v_xor_b32 v19, 32, v16\n"
    "v_xor_b32 v20, 64, v16\n"
    "v_xor_b32 v21, 96, v16\n"
    "s_setprio 3\n"
    "s_nop 5\n"
    "v_exp_f32 v56, v56\n"
    "v_exp_f32 v57, v57\n"
    "v_exp_f32 v58, v58\n"
    "s_mov_b64 exec, s[34:35]\n"
    "buffer_load_dwordx4 v[236:239], v3, s[44:47], s79 offen offset:3072 sc1\n"
    "buffer_load_dwordx4 v[240:243], v3, s[44:47], s79 offen offset:3584 sc1\n"
    "s_mov_b64 exec, -1\n"
    "v_exp_f32 v59, v59\n"
    "v_exp_f32 v60, v60\n"
    "v_mfma_f32_32x32x16_bf16 v[72:87], v[244:247], v[28:31], v[40:55]\n"
    "v_exp_f32 v61, v61\n"
    "v_exp_f32 v62, v62\n"
    "v_exp_f32 v63, v63\n"
    "v_exp_f32 v64, v64\n"
    "v_exp_f32 v65, v65\n"
    "s_waitcnt vmcnt(4)\n"
    "v_smfmac_f32_32x32x32_bf16 v[88:103], v[248:251], v[56:63], v4\n"
    "v_exp_f32 v66, v66\n"
    "v_exp_f32 v67, v67\n"
    "buffer_store_dwordx4 v[184:187], v1, s[44:47], s81 offen sc1\n"
    "v_exp_f32 v68, v68\n"
    "v_exp_f32 v69, v69\n"
    "v_mfma_f32_32x32x16_bf16 v[104:119], v[192:195], v[192:195], 0\n"
    "v_exp_f32 v70, v70\n"
    "v_exp_f32 v71, v71\n"
    "v_exp_f32 v72, v72\n"
    "v_exp_f32 v73, v73\n"
    "v_smfmac_f32_32x32x32_bf16 v[88:103], v[252:255], v[64:71], v4\n"
    "v_exp_f32 v74, v74\n"
    "v_exp_f32 v75, v75\n"
    "v_exp_f32 v76, v76\n"
    "v_mfma_f32_32x32x16_bf16 v[56:71], v[244:247], v[32:35], v[40:55]\n"
    "v_exp_f32 v77, v77\n"
    "v_exp_f32 v78, v78\n"
    "v_exp_f32 v79, v79\n"
    "v_exp_f32 v80, v80\n"
    "v_exp_f32 v81, v81\n"
    "v_smfmac_f32_32x32x32_bf16 v[104:119], v[248:251], v[72:79], v4\n"
    "v_exp_f32 v82, v82\n"
    "v_exp_f32 v83, v83\n"
    "s_add_u32 s81, s81, 57344\n"
    "buffer_store_dwordx4 v[188:191], v1, s[44:47], s81 offen sc1\n"
    "v_exp_f32 v84, v84\n"
    "v_exp_f32 v85, v85\n"
    "v_mfma_f32_32x32x16_bf16 v[120:135], v[192:195], v[192:195], 0\n"
    "v_exp_f32 v86, v86\n"
    "v_exp_f32 v87, v87\n"
    "v_exp_f32 v56, v56\n"
    "v_exp_f32 v57, v57\n"
    "v_smfmac_f32_32x32x32_bf16 v[104:119], v[252:255], v[80:87], v4\n"
    "v_exp_f32 v58, v58\n"
    "v_exp_f32 v59, v59\n"
    "v_exp_f32 v60, v60\n"
    "v_mfma_f32_32x32x16_bf16 v[72:87], v[244:247], v[36:39], v[40:55]\n"
    "v_exp_f32 v61, v61\n"
    "s_add_u32 s79, s77, s78\n"
    "s_add_u32 s78, s78, 4096\n"
    "buffer_load_dwordx4 v[244:247], v1, s[44:47], s79 offen offset:0 sc1\n"
    "s_and_b32 s78, s78, 32767\n"
    "v_exp_f32 v62, v62\n"
    "v_exp_f32 v63, v63\n"
    "v_exp_f32 v64, v64\n"
    "v_exp_f32 v65, v65\n"
    "v_smfmac_f32_32x32x32_bf16 v[120:135], v[248:251], v[56:63], v4\n"
    "v_exp_f32 v66, v66\n"
    "v_exp_f32 v67, v67\n"
    "v_exp_f32 v68, v68\n"
    "v_exp_f32 v69, v69\n"
    "v_mfma_f32_32x32x16_bf16 v[136:151], v[192:195], v[192:195], 0\n"
    "v_exp_f32 v70, v70\n"
    "v_exp_f32 v71, v71\n"
    "v_exp_f32 v72, v72\n"
    "v_exp_f32 v73, v73\n"
    "v_smfmac_f32_32x32x32_bf16 v[120:135], v[252:255], v[64:71], v4\n"
    "v_exp_f32 v74, v74\n"
    "v_exp_f32 v75, v75\n"
    "v_exp_f32 v76, v76\n"
    "s_waitcnt vmcnt(6)\n"
    "v_mfma_f32_32x32x16_bf16 v[56:71], v[232:235], v[24:27], v[40:55]\n"
    "v_exp_f32 v77, v77\n"
    "v_exp_f32 v78, v78\n"
    "v_exp_f32 v79, v79\n"
    "v_exp_f32 v80, v80\n"
    "v_exp_f32 v81, v81\n"
    "v_smfmac_f32_32x32x32_bf16 v[136:151], v[248:251], v[72:79], v4\n"
    "v_exp_f32 v82, v82\n"
    "v_exp_f32 v83, v83\n"
    "v_exp_f32 v84, v84\n"
    "v_exp_f32 v85, v85\n"
    "v_exp_f32 v86, v86\n"
    "v_exp_f32 v87, v87\n"
    "v_exp_f32 v56, v56\n"
    "v_exp_f32 v57, v57\n"
    "v_smfmac_f32_32x32x32_bf16 v[136:151], v[252:255], v[80:87], v4\n"
    "v_exp_f32 v58, v58\n"
    "s_mov_b64 exec, s[34:35]\n"
    "buffer_load_dwordx4 v[248:251], v3, s[44:47], s79 offen offset:1024 sc1\n"
    "buffer_load_dwordx4 v[252:255], v3, s[44:47], s79 offen offset:1536 sc1\n"
    "s_mov_b64 exec, -1\n"
    "v_exp_f32 v59, v59\n"
    "v_exp_f32 v60, v60\n"
    "v_mfma_f32_32x32x16_bf16 v[72:87], v[232:235], v[28:31], v[40:55]\n"
    "v_exp_f32 v61, v61\n"
    "v_exp_f32 v62, v62\n"
    "v_exp_f32 v63, v63\n"
    "v_exp_f32 v64, v64\n"
    "v_exp_f32 v65, v65\n"
    "s_waitcnt vmcnt(5)\n"
    "v_smfmac_f32_32x32x32_bf16 v[88:103], v[236:239], v[56:63], v4\n"
    "v_exp_f32 v66, v66\n"
    "v_exp_f32 v67, v67\n"
    "v_exp_f32 v68, v68\n"
    "v_exp_f32 v69, v69\n"
    "v_exp_f32 v70, v70\n"
    "v_exp_f32 v71, v71\n"
    "v_exp_f32 v72, v72\n"
    "v_exp_f32 v73, v73\n"
    "v_smfmac_f32_32x32x32_bf16 v[88:103], v[240:243], v[64:71], v4\n"
    "v_exp_f32 v74, v74\n"
    "v_exp_f32 v75, v75\n"
    "v_exp_f32 v76, v76\n"
    "v_mfma_f32_32x32x16_bf16 v[56:71], v[232:235], v[32:35], v[40:55]\n"
    "v_exp_f32 v77, v77\n"
    "v_exp_f32 v78, v78\n"
    "v_exp_f32 v79, v79\n"
    "v_exp_f32 v80, v80\n"
    "v_exp_f32 v81, v81\n"
    "v_smfmac_f32_32x32x32_bf16 v[104:119], v[236:239], v[72:79], v4\n"
    "v_exp_f32 v82, v82\n"
    "v_exp_f32 v83, v83\n"
    "v_exp_f32 v84, v84\n"
    "v_exp_f32 v85, v85\n"
    "v_exp_f32 v86, v86\n"
    "v_exp_f32 v87, v87\n"
    "v_exp_f32 v56, v56\n"
    "v_exp_f32 v57, v57\n"
    "v_smfmac_f32_32x32x32_bf16 v[104:119], v[240:243], v[80:87], v4\n"
    "v_exp_f32 v58, v58\n"
    "v_exp_f32 v59, v59\n"
    "v_exp_f32 v60, v60\n"
    "v_mfma_f32_32x32x16_bf16 v[72:87], v[232:235], v[36:39], v[40:55]\n"
    "v_exp_f32 v61, v61\n"
    "buffer_load_dwordx4 v[232:235], v1, s[44:47], s79 offen offset:2048 sc1\n"
    "v_exp_f32 v62, v62\n"
    "v_exp_f32 v63, v63\n"
    "v_exp_f32 v64, v64\n"
    "v_exp_f32 v65, v65\n"
    "v_smfmac_f32_32x32x32_bf16 v[120:135], v[236:239], v[56:63], v4\n"
    "v_exp_f32 v66, v66\n"
    "v_exp_f32 v67, v67\n"
    "v_exp_f32 v68, v68\n"
    "v_exp_f32 v69, v69\n"
    "v_exp_f32 v70, v70\n"
    "v_exp_f32 v71, v71\n"
    "v_exp_f32 v72, v72\n"
    "v_exp_f32 v73, v73\n"
    "v_smfmac_f32_32x32x32_bf16 v[120:135], v[240:243], v[64:71], v4\n"
    "v_exp_f32 v74, v74\n"
    "v_exp_f32 v75, v75\n"
    "v_exp_f32 v76, v76\n"
    "s_waitcnt vmcnt(3)\n"
    "v_mfma_f32_32x32x16_bf16 v[56:71], v[244:247], v[24:27], v[40:55]\n"
    "v_exp_f32 v77, v77\n"
    "v_exp_f32 v78, v78\n"
    "v_exp_f32 v79, v79\n"
    "v_exp_f32 v80, v80\n"
    "v_exp_f32 v81, v81\n"
    "v_smfmac_f32_32x32x32_bf16 v[136:151], v[236:239], v[72:79], v4\n"
    "v_exp_f32 v82, v82\n"
    "v_exp_f32 v83, v83\n"
    "v_mov_b64 v[10:11], s[22:23]\n"
    "s_mov_b64 exec, 1\n"
    "buffer_store_dwordx2 v[10:11], v1, s[44:47], s82 offen sc1\n"
    "s_mov_b64 exec, -1\n"
    "v_exp_f32 v84, v84\n"
    "v_exp_f32 v85, v85\n"
    "v_exp_f32 v86, v86\n"
    "v_exp_f32 v87, v87\n"
    "s_setprio 2\n"
    "v_exp_f32 v56, v56\n"
    "v_exp_f32 v57, v57\n"
    "v_smfmac_f32_32x32x32_bf16 v[136:151], v[240:243], v[80:87], v4\n"
    "v_exp_f32 v58, v58\n"
    "s_mov_b64 exec, s[34:35]\n"
    "buffer_load_dwordx4 v[236:239], v3, s[44:47], s79 offen offset:3072 sc1\n"
    "buffer_load_dwordx4 v[240:243], v3, s[44:47], s79 offen offset:3584 sc1\n"
    "s_mov_b64 exec, -1\n"
    "v_exp_f32 v59, v59\n"
    "v_exp_f32 v60, v60\n"
    "v_mfma_f32_32x32x16_bf16 v[72:87], v[244:247], v[28:31], v[40:55]\n"
    "v_exp_f32 v61, v61\n"
    "v_exp_f32 v62, v62\n"
    "v_exp_f32 v63, v63\n"
    "v_exp_f32 v64, v64\n"
    "v_exp_f32 v65, v65\n"
    "s_waitcnt vmcnt(4)\n"
    "v_smfmac_f32_32x32x32_bf16 v[88:103], v[248:251], v[56:63], v4\n"
    "v_exp_f32 v66, v66\n"
    "v_exp_f32 v67, v67\n"
    "v_exp_f32 v68, v68\n"
    "v_exp_f32 v69, v69\n"
    "v_exp_f32 v70, v70\n"
    "v_exp_f32 v71, v71\n"
    "v_exp_f32 v72, v72\n"
    "v_exp_f32 v73, v73\n"
    "v_smfmac_f32_32x32x32_bf16 v[88:103], v[252:255], v[64:71], v4\n"
    "v_exp_f32 v74, v74\n"
    "v_exp_f32 v75, v75\n"
    "v_exp_f32 v76, v76\n"
    "v_mfma_f32_32x32x16_bf16 v[56:71], v[244:247], v[32:35], v[40:55]\n"
    "v_exp_f32 v77, v77\n"
    "v_exp_f32 v78, v78\n"
    "v_exp_f32 v79, v79\n"
    "v_exp_f32 v80, v80\n"
    "v_exp_f32 v81, v81\n"
    "v_smfmac_f32_32x32x32_bf16 v[104:119], v[248:251], v[72:79], v4\n"
    "v_exp_f32 v82, v82\n"
    "v_exp_f32 v83, v83\n"
    "s_add_u32 m0, s87, 0\n"
    "s_add_u32 s61, s96, 0\n"
    "buffer_load_dwordx4 v5, s[24:27], s61 offen lds\n"
    "v_exp_f32 v84, v84\n"
    "v_exp_f32 v85, v85\n"
    "v_exp_f32 v86, v86\n"
    "v_exp_f32 v87, v87\n"
    "v_exp_f32 v56, v56\n"
    "v_exp_f32 v57, v57\n"
    "v_smfmac_f32_32x32x32_bf16 v[104:119], v[252:255], v[80:87], v4\n"
    "v_exp_f32 v58, v58\n"
    "v_exp_f32 v59, v59\n"
    "v_exp_f32 v60, v60\n"
    "v_mfma_f32_32x32x16_bf16 v[72:87], v[244:247], v[36:39], v[40:55]\n"
    "v_exp_f32 v61, v61\n"
    "s_add_u32 s79, s77, s78\n"
    "s_add_u32 s78, s78, 4096\n"
    "buffer_load_dwordx4 v[244:247], v1, s[44:47], s79 offen offset:0 sc1\n"
    "s_and_b32 s78, s78, 32767\n"
    "v_exp_f32 v62, v62\n"
    "v_exp_f32 v63, v63\n"
    "v_exp_f32 v64, v64\n"
    "v_exp_f32 v65, v65\n"
    "v_smfmac_f32_32x32x32_bf16 v[120:135], v[248:251], v[56:63], v4\n"
    "v_exp_f32 v66, v66\n"
    "v_exp_f32 v67, v67\n"
    "s_add_u32 m0, s87, 1024\n"
    "s_add_u32 s61, s96, 8192\n"
    "buffer_load_dwordx4 v5, s[24:27], s61 offen lds\n"
    "v_exp_f32 v68, v68\n"
    "v_exp_f32 v69, v69\n"
    "v_exp_f32 v70, v70\n"
    "v_exp_f32 v71, v71\n"
    "v_exp_f32 v72, v72\n"
    "v_exp_f32 v73, v73\n"
    "v_smfmac_f32_32x32x32_bf16 v[120:135], v[252:255], v[64:71], v4\n"
    "v_exp_f32 v74, v74\n"
    "v_exp_f32 v75, v75\n"
    "v_exp_f32 v76, v76\n"
    "s_waitcnt vmcnt(6)\n"
    "v_mfma_f32_32x32x16_bf16 v[56:71], v[232:235], v[24:27], v[40:55]\n"
    "v_exp_f32 v77, v77\n"
    "v_exp_f32 v78, v78\n"
    "v_exp_f32 v79, v79\n"
    "v_exp_f32 v80, v80\n"
    "v_exp_f32 v81, v81\n"
    "v_smfmac_f32_32x32x32_bf16 v[136:151], v[248:251], v[72:79], v4\n"
    "v_exp_f32 v82, v82\n"
    "v_exp_f32 v83, v83\n"
    "s_add_u32 m0, s87, 4096\n"
    "s_add_u32 s61, s96, 32768\n"
    "buffer_load_dwordx4 v5, s[24:27], s61 offen lds\n"
    "v_exp_f32 v84, v84\n"
    "v_exp_f32 v85, v85\n"
    "v_exp_f32 v86, v86\n"
    "v_exp_f32 v87, v87\n"
    "v_exp_f32 v56, v56\n"
    "v_exp_f32 v57, v57\n"
    "v_smfmac_f32_32x32x32_bf16 v[136:151], v[252:255], v[80:87], v4\n"
    "v_exp_f32 v58, v58\n"
    "s_mov_b64 exec, s[34:35]\n"
    "buffer_load_dwordx4 v[248:251], v3, s[44:47], s79 offen offset:1024 sc1\n"
    "buffer_load_dwordx4 v[252:255], v3, s[44:47], s79 offen offset:1536 sc1\n"
    "s_mov_b64 exec, -1\n"
    "v_exp_f32 v59, v59\n"
    "v_exp_f32 v60, v60\n"
    "v_mfma_f32_32x32x16_bf16 v[72:87], v[232:235], v[28:31], v[40:55]\n"
    "v_exp_f32 v61, v61\n"
    "v_exp_f32 v62, v62\n"
    "v_exp_f32 v63, v63\n"
    "v_exp_f32 v64, v64\n"
    "v_exp_f32 v65, v65\n"
    "s_waitcnt vmcnt(6)\n"
    "v_smfmac_f32_32x32x32_bf16 v[88:103], v[236:239], v[56:63], v4\n"
    "v_exp_f32 v66, v66\n"
    "v_exp_f32 v67, v67\n"
    "s_add_u32 m0, s87, 5120\n"
    "s_add_u32 s61, s96, 40960\n"
    "buffer_load_dwordx4 v5, s[24:27], s61 offen lds\n"
    "v_exp_f32 v68, v68\n"
    "v_exp_f32 v69, v69\n"
    "v_exp_f32 v70, v70\n"
    "v_exp_f32 v71, v71\n"
    "v_exp_f32 v72, v72\n"
    "v_exp_f32 v73, v73\n"
    "v_smfmac_f32_32x32x32_bf16 v[88:103], v[240:243], v[64:71], v4\n"
    "v_exp_f32 v74, v74\n"
    "v_exp_f32 v75, v75\n"
    "v_exp_f32 v76, v76\n"
    "v_mfma_f32_32x32x16_bf16 v[56:71], v[232:235], v[32:35], v[40:55]\n"
    "v_exp_f32 v77, v77\n"
    "v_exp_f32 v78, v78\n"
    "v_exp_f32 v79, v79\n"
    "v_exp_f32 v80, v80\n"
    "v_exp_f32 v81, v81\n"
    "v_smfmac_f32_32x32x32_bf16 v[104:119], v[236:239], v[72:79], v4\n"
    "v_exp_f32 v82, v82\n"
    "v_exp_f32 v83, v83\n"
    "s_add_u32 m0, s87, 8192\n"
    "s_add_u32 s61, s96, 65536\n"
    "buffer_load_dwordx4 v5, s[24:27], s61 offen lds\n"
    "v_exp_f32 v84, v84\n"
    "v_exp_f32 v85, v85\n"
    "v_exp_f32 v86, v86\n"
    "v_exp_f32 v87, v87\n"
    "v_exp_f32 v56, v56\n"
    "v_exp_f32 v57, v57\n"
    "v_smfmac_f32_32x32x32_bf16 v[104:119], v[240:243], v[80:87], v4\n"
    "v_exp_f32 v58, v58\n"
    "v_exp_f32 v59, v59\n"
    "v_exp_f32 v60, v60\n"
    "v_mfma_f32_32x32x16_bf16 v[72:87], v[232:235], v[36:39], v[40:55]\n"
    "v_exp_f32 v61, v61\n"
    "buffer_load_dwordx4 v[232:235], v1, s[44:47], s79 offen offset:2048 sc1\n"
    "v_exp_f32 v62, v62\n"
    "v_exp_f32 v63, v63\n"
    "v_exp_f32 v64, v64\n"
    "v_exp_f32 v65, v65\n"
    "v_smfmac_f32_32x32x32_bf16 v[120:135], v[236:239], v[56:63], v4\n"
    "v_exp_f32 v66, v66\n"
    "v_exp_f32 v67, v67\n"
    "s_add_u32 m0, s87, 9216\n"
    "s_add_u32 s61, s96, 73728\n"
    "buffer_load_dwordx4 v5, s[24:27], s61 offen lds\n"
    "v_exp_f32 v68, v68\n"
    "v_exp_f32 v69, v69\n"
    "v_exp_f32 v70, v70\n"
    "v_exp_f32 v71, v71\n"
    "v_exp_f32 v72, v72\n"
    "v_exp_f32 v73, v73\n"
    "v_smfmac_f32_32x32x32_bf16 v[120:135], v[240:243], v[64:71], v4\n"
    "v_exp_f32 v74, v74\n"
    "v_exp_f32 v75, v75\n"
    "v_exp_f32 v76, v76\n"
    "s_waitcnt vmcnt(8)\n"
    "v_mfma_f32_32x32x16_bf16 v[56:71], v[244:247], v[24:27], v[40:55]\n"
    "v_exp_f32 v77, v77\n"
    "v_exp_f32 v78, v78\n"
    "v_exp_f32 v79, v79\n"
    "v_exp_f32 v80, v80\n"
    "v_exp_f32 v81, v81\n"
    "v_smfmac_f32_32x32x32_bf16 v[136:151], v[236:239], v[72:79], v4\n"
    "v_exp_f32 v82, v82\n"
    "v_exp_f32 v83, v83\n"
    "s_add_u32 m0, s87, 12288\n"
    "s_add_u32 s61, s96, 98304\n"
    "buffer_load_dwordx4 v5, s[24:27], s61 offen lds\n"
    "v_exp_f32 v84, v84\n"
    "v_exp_f32 v85, v85\n"
    "v_exp_f32 v86, v86\n"
    "v_exp_f32 v87, v87\n"
    "v_exp_f32 v56, v56\n"
    "v_exp_f32 v57, v57\n"
    "v_smfmac_f32_32x32x32_bf16 v[136:151], v[240:243], v[80:87], v4\n"
    "v_exp_f32 v58, v58\n"
    "s_mov_b64 exec, s[34:35]\n"
    "buffer_load_dwordx4 v[236:239], v3, s[44:47], s79 offen offset:3072 sc1\n"
    "buffer_load_dwordx4 v[240:243], v3, s[44:47], s79 offen offset:3584 sc1\n"
    "s_mov_b64 exec, -1\n"
    "v_exp_f32 v59, v59\n"
    "v_exp_f32 v60, v60\n"
    "v_mfma_f32_32x32x16_bf16 v[72:87], v[244:247], v[28:31], v[40:55]\n"
    "v_exp_f32 v61, v61\n"
    "v_exp_f32 v62, v62\n"
    "v_exp_f32 v63, v63\n"
    "v_exp_f32 v64, v64\n"
    "v_exp_f32 v65, v65\n"
    "s_waitcnt vmcnt(7)\n"
    "v_smfmac_f32_32x32x32_bf16 v[88:103], v[248:251], v[56:63], v4\n"
    "v_exp_f32 v66, v66\n"
    "v_exp_f32 v67, v67\n"
    "s_add_u32 m0, s87, 13312\n"
    "s_add_u32 s61, s96, 106496\n"
    "buffer_load_dwordx4 v5, s[24:27], s61 offen lds\n"
    "v_exp_f32 v68, v68\n"
    "v_exp_f32 v69, v69\n"
    "v_exp_f32 v70, v70\n"
    "v_exp_f32 v71, v71\n"
    "v_exp_f32 v72, v72\n"
    "v_exp_f32 v73, v73\n"
    "v_smfmac_f32_32x32x32_bf16 v[88:103], v[252:255], v[64:71], v4\n"
    "v_exp_f32 v74, v74\n"
    "v_exp_f32 v75, v75\n"
    "v_exp_f32 v76, v76\n"
    "v_mfma_f32_32x32x16_bf16 v[56:71], v[244:247], v[32:35], v[40:55]\n"
    "v_exp_f32 v77, v77\n"
    "v_exp_f32 v78, v78\n"
    "v_exp_f32 v79, v79\n"
    "v_exp_f32 v80, v80\n"
    "v_exp_f32 v81, v81\n"
    "v_smfmac_f32_32x32x32_bf16 v[104:119], v[248:251], v[72:79], v4\n"
    "v_exp_f32 v82, v82\n"
    "v_exp_f32 v83, v83\n"
    "s_add_u32 m0, s87, 16384\n"
    "s_add_u32 s61, s96, 131072\n"
    "buffer_load_dwordx4 v5, s[24:27], s61 offen lds\n"
    "v_exp_f32 v84, v84\n"
    "v_exp_f32 v85, v85\n"
    "v_exp_f32 v86, v86\n"
    "v_exp_f32 v87, v87\n"
    "v_exp_f32 v56, v56\n"
    "v_exp_f32 v57, v57\n"
    "v_smfmac_f32_32x32x32_bf16 v[104:119], v[252:255], v[80:87], v4\n"
    "v_exp_f32 v58, v58\n"
    "v_exp_f32 v59, v59\n"
    "v_exp_f32 v60, v60\n"
    "v_mfma_f32_32x32x16_bf16 v[72:87], v[244:247], v[36:39], v[40:55]\n"
    "v_exp_f32 v61, v61\n"
    "s_add_u32 s79, s77, s78\n"
    "s_add_u32 s78, s78, 4096\n"
    "buffer_load_dwordx4 v[244:247], v1, s[44:47], s79 offen offset:0 sc1\n"
    "s_and_b32 s78, s78, 32767\n"
    "v_exp_f32 v62, v62\n"
    "v_exp_f32 v63, v63\n"
    "v_exp_f32 v64, v64\n"
    "v_exp_f32 v65, v65\n"
    "v_smfmac_f32_32x32x32_bf16 v[120:135], v[248:251], v[56:63], v4\n"
    "v_exp_f32 v66, v66\n"
    "v_exp_f32 v67, v67\n"
    "s_add_u32 m0, s87, 17408\n"
    "s_add_u32 s61, s96, 139264\n"
    "buffer_load_dwordx4 v5, s[24:27], s61 offen lds\n"
    "v_exp_f32 v68, v68\n"
    "v_exp_f32 v69, v69\n"
    "v_exp_f32 v70, v70\n"
    "v_exp_f32 v71, v71\n"
    "v_exp_f32 v72, v72\n"
    "v_exp_f32 v73, v73\n"
    "v_smfmac_f32_32x32x32_bf16 v[120:135], v[252:255], v[64:71], v4\n"
    "v_exp_f32 v74, v74\n"
    "v_exp_f32 v75, v75\n"
    "v_exp_f32 v76, v76\n"
    "s_waitcnt vmcnt(8)\n"
    "v_mfma_f32_32x32x16_bf16 v[56:71], v[232:235], v[24:27], v[40:55]\n"
    "v_exp_f32 v77, v77\n"
    "v_exp_f32 v78, v78\n"
    "v_exp_f32 v79, v79\n"
    "v_exp_f32 v80, v80\n"
    "v_exp_f32 v81, v81\n"
    "v_smfmac_f32_32x32x32_bf16 v[136:151], v[248:251], v[72:79], v4\n"
    "v_exp_f32 v82, v82\n"
    "v_exp_f32 v83, v83\n"
    "s_add_u32 m0, s87, 20480\n"
    "s_add_u32 s61, s96, 163840\n"
    "buffer_load_dwordx4 v5, s[24:27], s61 offen lds\n"
    "v_exp_f32 v84, v84\n"
    "v_exp_f32 v85, v85\n"
    "v_exp_f32 v86, v86\n"
    "v_exp_f32 v87, v87\n"
    "v_exp_f32 v56, v56\n"
    "v_exp_f32 v57, v57\n"
    "v_smfmac_f32_32x32x32_bf16 v[136:151], v[252:255], v[80:87], v4\n"
    "v_exp_f32 v58, v58\n"
    "s_mov_b64 exec, s[34:35]\n"
    "buffer_load_dwordx4 v[248:251], v3, s[44:47], s79 offen offset:1024 sc1\n"
    "buffer_load_dwordx4 v[252:255], v3, s[44:47], s79 offen offset:1536 sc1\n"
    "s_mov_b64 exec, -1\n"
    "v_exp_f32 v59, v59\n"
    "v_exp_f32 v60, v60\n"
    "v_mfma_f32_32x32x16_bf16 v[72:87], v[232:235], v[28:31], v[40:55]\n"
    "v_exp_f32 v61, v61\n"
    "v_exp_f32 v62, v62\n"
    "v_exp_f32 v63, v63\n"
    "v_exp_f32 v64, v64\n"
    "v_exp_f32 v65, v65\n"
    "s_waitcnt vmcnt(7)\n"
    "v_smfmac_f32_32x32x32_bf16 v[88:103], v[236:239], v[56:63], v4\n"
    "v_exp_f32 v66, v66\n"
    "v_exp_f32 v67, v67\n"
    "s_add_u32 m0, s87, 21504\n"
    "s_add_u32 s61, s96, 172032\n"
    "buffer_load_dwordx4 v5, s[24:27], s61 offen lds\n"
    "v_exp_f32 v68, v68\n"
    "v_exp_f32 v69, v69\n"
    "v_exp_f32 v70, v70\n"
    "v_exp_f32 v71, v71\n"
    "v_exp_f32 v72, v72\n"
    "v_exp_f32 v73, v73\n"
    "v_smfmac_f32_32x32x32_bf16 v[88:103], v[240:243], v[64:71], v4\n"
    "v_exp_f32 v74, v74\n"
    "v_exp_f32 v75, v75\n"
    "v_exp_f32 v76, v76\n"
    "v_mfma_f32_32x32x16_bf16 v[56:71], v[232:235], v[32:35], v[40:55]\n"
    "v_exp_f32 v77, v77\n"
    "v_exp_f32 v78, v78\n"
    "v_exp_f32 v79, v79\n"
    "v_exp_f32 v80, v80\n"
    "v_exp_f32 v81, v81\n"
    "v_smfmac_f32_32x32x32_bf16 v[104:119], v[236:239], v[72:79], v4\n"
    "v_exp_f32 v82, v82\n"
    "v_exp_f32 v83, v83\n"
    "buffer_load_dwordx4 v[8:11], v14, s[44:47], 0 offen sc1\n"
    "v_exp_f32 v84, v84\n"
    "v_exp_f32 v85, v85\n"
    "v_exp_f32 v86, v86\n"
    "v_exp_f32 v87, v87\n"
    "v_exp_f32 v56, v56\n"
    "v_exp_f32 v57, v57\n"
    "v_smfmac_f32_32x32x32_bf16 v[104:119], v[240:243], v[80:87], v4\n"
    "v_exp_f32 v58, v58\n"
    "v_exp_f32 v59, v59\n"
    "v_exp_f32 v60, v60\n"
    "v_mfma_f32_32x32x16_bf16 v[72:87], v[232:235], v[36:39], v[40:55]\n"
    "v_exp_f32 v61, v61\n"
    "buffer_load_dwordx4 v[232:235], v1, s[44:47], s79 offen offset:2048 sc1\n"
    "v_exp_f32 v62, v62\n"
    "v_exp_f32 v63, v63\n"
    "v_exp_f32 v64, v64\n"
    "v_exp_f32 v65, v65\n"
    "v_smfmac_f32_32x32x32_bf16 v[120:135], v[236:239], v[56:63], v4\n"
    "v_exp_f32 v66, v66\n"
    "v_exp_f32 v67, v67\n"
    "v_exp_f32 v68, v68\n"
    "v_exp_f32 v69, v69\n"
    "v_exp_f32 v70, v70\n"
    "v_exp_f32 v71, v71\n"
    "v_exp_f32 v72, v72\n"
    "v_exp_f32 v73, v73\n"
    "v_smfmac_f32_32x32x32_bf16 v[120:135], v[240:243], v[64:71], v4\n"
    "v_exp_f32 v74, v74\n"
    "v_exp_f32 v75, v75\n"
    "v_exp_f32 v76, v76\n"
    "s_waitcnt vmcnt(7)\n"
    "v_mfma_f32_32x32x16_bf16 v[56:71], v[244:247], v[24:27], v[40:55]\n"
    "v_exp_f32 v77, v77\n"
    "v_exp_f32 v78, v78\n"
    "v_exp_f32 v79, v79\n"
    "v_exp_f32 v80, v80\n"
    "v_exp_f32 v81, v81\n"
    "v_smfmac_f32_32x32x32_bf16 v[136:151], v[236:239], v[72:79], v4\n"
    "v_exp_f32 v82, v82\n"
    "v_exp_f32 v83, v83\n"
    "v_exp_f32 v84, v84\n"
    "v_exp_f32 v85, v85\n"
    "v_exp_f32 v86, v86\n"
    "v_exp_f32 v87, v87\n"
    "v_exp_f32 v56, v56\n"
    "v_exp_f32 v57, v57\n"
    "v_smfmac_f32_32x32x32_bf16 v[136:151], v[240:243], v[80:87], v4\n"
    "v_exp_f32 v58, v58\n"
    "s_mov_b64 exec, s[34:35]\n"
    "buffer_load_dwordx4 v[236:239], v3, s[44:47], s79 offen offset:3072 sc1\n"
    "buffer_load_dwordx4 v[240:243], v3, s[44:47], s79 offen offset:3584 sc1\n"
    "s_mov_b64 exec, -1\n"
    "v_exp_f32 v59, v59\n"
    "v_exp_f32 v60, v60\n"
    "v_mfma_f32_32x32x16_bf16 v[72:87], v[244:247], v[28:31], v[40:55]\n"
    "v_exp_f32 v61, v61\n"
    "v_exp_f32 v62, v62\n"
    "v_exp_f32 v63, v63\n"
    "v_exp_f32 v64, v64\n"
    "v_exp_f32 v65, v65\n"
    "s_waitcnt vmcnt(5)\n"
    "v_smfmac_f32_32x32x32_bf16 v[88:103], v[248:251], v[56:63], v4\n"
    "v_exp_f32 v66, v66\n"
    "v_exp_f32 v67, v67\n"
    "v_exp_f32 v68, v68\n"
    "v_exp_f32 v69, v69\n"
    "v_exp_f32 v70, v70\n"
    "v_exp_f32 v71, v71\n"
    "v_exp_f32 v72, v72\n"
    "v_exp_f32 v73, v73\n"
    "v_smfmac_f32_32x32x32_bf16 v[88:103], v[252:255], v[64:71], v4\n"
    "v_exp_f32 v74, v74\n"
    "v_exp_f32 v75, v75\n"
    "v_exp_f32 v76, v76\n"
    "v_mfma_f32_32x32x16_bf16 v[56:71], v[244:247], v[32:35], v[40:55]\n"
    "v_exp_f32 v77, v77\n"
    "v_exp_f32 v78, v78\n"
    "v_exp_f32 v79, v79\n"
    "v_exp_f32 v80, v80\n"
    "v_exp_f32 v81, v81\n"
    "v_smfmac_f32_32x32x32_bf16 v[104:119], v[248:251], v[72:79], v4\n"
    "v_exp_f32 v82, v82\n"
    "v_exp_f32 v83, v83\n"
    "v_exp_f32 v84, v84\n"
    "v_exp_f32 v85, v85\n"
    "v_exp_f32 v86, v86\n"
    "v_exp_f32 v87, v87\n"
    "v_exp_f32 v56, v56\n"
    "v_exp_f32 v57, v57\n"
    "v_smfmac_f32_32x32x32_bf16 v[104:119], v[252:255], v[80:87], v4\n"
    "v_exp_f32 v58, v58\n"
    "v_exp_f32 v59, v59\n"
    "v_exp_f32 v60, v60\n"
    "v_mfma_f32_32x32x16_bf16 v[72:87], v[244:247], v[36:39], v[40:55]\n"
    "v_exp_f32 v61, v61\n"
    "s_add_u32 s79, s77, s78\n"
    "s_add_u32 s78, s78, 4096\n"
    "buffer_load_dwordx4 v[244:247], v1, s[44:47], s79 offen offset:0 sc1\n"
    "s_and_b32 s78, s78, 32767\n"
    "v_exp_f32 v62, v62\n"
    "v_exp_f32 v63, v63\n"
    "v_exp_f32 v64, v64\n"
    "v_exp_f32 v65, v65\n"
    "v_smfmac_f32_32x32x32_bf16 v[120:135], v[248:251], v[56:63], v4\n"
    "v_exp_f32 v66, v66\n"
    "v_exp_f32 v67, v67\n"
    "v_exp_f32 v68, v68\n"
    "v_exp_f32 v69, v69\n"
    "v_exp_f32 v70, v70\n"
    "v_exp_f32 v71, v71\n"
    "v_exp_f32 v72, v72\n"
    "v_exp_f32 v73, v73\n"
    "v_smfmac_f32_32x32x32_bf16 v[120:135], v[252:255], v[64:71], v4\n"
    "v_exp_f32 v74, v74\n"
    "v_exp_f32 v75, v75\n"
    "v_exp_f32 v76, v76\n"
    "s_waitcnt vmcnt(3)\n"
    "v_mfma_f32_32x32x16_bf16 v[56:71], v[232:235], v[24:27], v[40:55]\n"
    "v_exp_f32 v77, v77\n"
    "v_exp_f32 v78, v78\n"
    "v_exp_f32 v79, v79\n"
    "v_exp_f32 v80, v80\n"
    "v_exp_f32 v81, v81\n"
    "v_smfmac_f32_32x32x32_bf16 v[136:151], v[248:251], v[72:79], v4\n"
    "v_exp_f32 v82, v82\n"
    "v_exp_f32 v83, v83\n"
    "v_cmp_ne_u64 vcc, s[22:23], v[8:9]\n"
    "v_cmp_ne_u64 s[48:49], s[22:23], v[10:11]\n"
    "s_nop 1\n"
    "s_or_b64 vcc, vcc, s[48:49]\n"
    "s_cbranch_vccnz LslowB_21_%=\n"
    "LbackB_22_%=:\n"
    "v_and_b32 v10, 15, v7\n"
    "v_lshlrev_b32 v10, 2, v10\n"
    "buffer_load_dword v6, v10, s[44:47], s84 offen offset:0 sc1\n"
    "buffer_load_dword v16, v10, s[44:47], s84 offen offset:64 sc1\n"
    "v_exp_f32 v84, v84\n"
    "v_exp_f32 v85, v85\n"
    "v_exp_f32 v86, v86\n"
    "v_exp_f32 v87, v87\n"
    "v_exp_f32 v56, v56\n"
    "v_exp_f32 v57, v57\n"
    "v_smfmac_f32_32x32x32_bf16 v[136:151], v[252:255], v[80:87], v4\n"
    "v_exp_f32 v58, v58\n"
    "s_mov_b64 exec, s[34:35]\n"
    "buffer_load_dwordx4 v[248:251], v3, s[44:47], s79 offen offset:1024 sc1\n"
    "buffer_load_dwordx4 v[252:255], v3, s[44:47], s79 offen offset:1536 sc1\n"
    "s_mov_b64 exec, -1\n"
    "v_exp_f32 v59, v59\n"
    "v_exp_f32 v60, v60\n"
    "v_mfma_f32_32x32x16_bf16 v[72:87], v[232:235], v[28:31], v[40:55]\n"
    "v_exp_f32 v61, v61\n"
    "v_exp_f32 v62, v62\n"
    "v_exp_f32 v63, v63\n"
    "v_exp_f32 v64, v64\n"
    "v_exp_f32 v65, v65\n"
    "s_waitcnt vmcnt(5)\n"
    "v_smfmac_f32_32x32x32_bf16 v[88:103], v[236:239], v[56:63], v4\n"
    "v_exp_f32 v66, v66\n"
    "v_exp_f32 v67, v67\n"
    "buffer_load_dwordx4 v[152:155], v1, s[44:47], s98 offen offset:0 sc1\n"
    "v_exp_f32 v68, v68\n"
    "v_exp_f32 v69, v69\n"
    "v_exp_f32 v70, v70\n"
    "v_exp_f32 v71, v71\n"
    "v_exp_f32 v72, v72\n"
    "v_exp_f32 v73, v73\n"
    "v_smfmac_f32_32x32x32_bf16 v[88:103], v[240:243], v[64:71], v4\n"
    "v_exp_f32 v74, v74\n"
    "v_exp_f32 v75, v75\n"
    "v_exp_f32 v76, v76\n"
    "v_mfma_f32_32x32x16_bf16 v[56:71], v[232:235], v[32:35], v[40:55]\n"
    "v_exp_f32 v77, v77\n"
    "v_exp_f32 v78, v78\n"
    "v_exp_f32 v79, v79\n"
    "v_exp_f32 v80, v80\n"
    "v_exp_f32 v81, v81\n"
    "v_smfmac_f32_32x32x32_bf16 v[104:119], v[236:239], v[72:79], v4\n"
    "v_exp_f32 v82, v82\n"
    "v_exp_f32 v83, v83\n"
    "s_add_u32 s61, s98, 57344\n"
    "buffer_load_dwordx4 v[156:159], v1, s[44:47], s61 offen offset:0 sc1\n"
    "s_or_b32 s61, %2, s52\n"
    "s_cbranch_scc0 Lepoch_23_%=\n"
    "LepochB_24_%=:\n"
    "v_exp_f32 v84, v84\n"
    "v_exp_f32 v85, v85\n"
    "v_exp_f32 v86, v86\n"
    "v_exp_f32 v87, v87\n"
    "v_exp_f32 v56, v56\n"
    "v_exp_f32 v57, v57\n"
    "v_smfmac_f32_32x32x32_bf16 v[104:119], v[240:243], v[80:87], v4\n"
    "v_exp_f32 v58, v58\n"
    "v_exp_f32 v59, v59\n"
    "v_exp_f32 v60, v60\n"
    "v_mfma_f32_32x32x16_bf16 v[72:87], v[232:235], v[36:39], v[40:55]\n"
    "v_exp_f32 v61, v61\n"
    "buffer_load_dwordx4 v[232:235], v1, s[44:47], s79 offen offset:2048 sc1\n"
    "v_exp_f32 v62, v62\n"
    "v_exp_f32 v63, v63\n"
    "v_exp_f32 v64, v64\n"
    "v_exp_f32 v65, v65\n"
    "v_smfmac_f32_32x32x32_bf16 v[120:135], v[236:239], v[56:63], v4\n"
    "v_exp_f32 v66, v66\n"
    "v_exp_f32 v67, v67\n"
    "buffer_load_dwordx4 v[160:163], v1, s[44:47], s98 offen offset:1024 sc1\n"
    "v_exp_f32 v68, v68\n"
    "v_exp_f32 v69, v69\n"
    "v_exp_f32 v70, v70\n"
    "v_exp_f32 v71, v71\n"
    "v_exp_f32 v72, v72\n"
    "v_exp_f32 v73, v73\n"
    "v_smfmac_f32_32x32x32_bf16 v[120:135], v[240:243], v[64:71], v4\n"
    "v_exp_f32 v74, v74\n"
    "v_exp_f32 v75, v75\n"
    "v_exp_f32 v76, v76\n"
    "s_waitcnt vmcnt(8)\n"
    "v_mfma_f32_32x32x16_bf16 v[56:71], v[244:247], v[24:27], v[40:55]\n"
    "v_exp_f32 v77, v77\n"
    "v_exp_f32 v78, v78\n"
    "v_exp_f32 v79, v79\n"
    "v_exp_f32 v80, v80\n"
    "v_exp_f32 v81, v81\n"
    "v_smfmac_f32_32x32x32_bf16 v[136:151], v[236:239], v[72:79], v4\n"
    "v_exp_f32 v82, v82\n"
    "v_exp_f32 v83, v83\n"
    "s_add_u32 s61, s98, 57344\n"
    "buffer_load_dwordx4 v[164:167], v1, s[44:47], s61 offen offset:1024 sc1\n"
    "v_exp_f32 v84, v84\n"
    "v_exp_f32 v85, v85\n"
    "v_exp_f32 v86, v86\n"
    "v_exp_f32 v87, v87\n"
    "s_setprio 1\n"
    "v_exp_f32 v56, v56\n"
    "v_exp_f32 v57, v57\n"
    "v_smfmac_f32_32x32x32_bf16 v[136:151], v[240:243], v[80:87], v4\n"
    "v_exp_f32 v58, v58\n"
    "s_mov_b64 exec, s[34:35]\n"
    "buffer_load_dwordx4 v[236:239], v3, s[44:47], s79 offen offset:3072 sc1\n"
    "buffer_load_dwordx4 v[240:243], v3, s[44:47], s79 offen offset:3584 sc1\n"
    "s_mov_b64 exec, -1\n"
    "v_exp_f32 v59, v59\n"
    "v_exp_f32 v60, v60\n"
    "v_mfma_f32_32x32x16_bf16 v[72:87], v[244:247], v[28:31], v[40:55]\n"
    "v_exp_f32 v61, v61\n"
    "v_exp_f32 v62, v62\n"
    "v_exp_f32 v63, v63\n"
    "v_exp_f32 v64, v64\n"
    "v_exp_f32 v65, v65\n"
    "s_waitcnt vmcnt(7)\n"
    "v_smfmac_f32_32x32x32_bf16 v[88:103], v[248:251], v[56:63], v4\n"
    "v_exp_f32 v66, v66\n"
    "v_exp_f32 v67, v67\n"
    "buffer_load_dwordx4 v[168:171], v1, s[44:47], s98 offen offset:2048 sc1\n"
    "v_exp_f32 v68, v68\n"
    "v_exp_f32 v69, v69\n"
    "v_exp_f32 v70, v70\n"
    "v_exp_f32 v71, v71\n"
    "v_exp_f32 v72, v72\n"
    "v_exp_f32 v73, v73\n"
    "v_smfmac_f32_32x32x32_bf16 v[88:103], v[252:255], v[64:71], v4\n"
    "v_exp_f32 v74, v74\n"
    "v_exp_f32 v75, v75\n"
    "v_exp_f32 v76, v76\n"
    "v_mfma_f32_32x32x16_bf16 v[56:71], v[244:247], v[32:35], v[40:55]\n"
    "v_exp_f32 v77, v77\n"
    "v_exp_f32 v78, v78\n"
    "v_exp_f32 v79, v79\n"
    "v_exp_f32 v80, v80\n"
    "v_exp_f32 v81, v81\n"
    "v_smfmac_f32_32x32x32_bf16 v[104:119], v[248:251], v[72:79], v4\n"
    "v_exp_f32 v82, v82\n"
    "v_exp_f32 v83, v83\n"
    "s_add_u32 s61, s98, 57344\n"
    "buffer_load_dwordx4 v[172:175], v1, s[44:47], s61 offen offset:2048 sc1\n"
    "v_exp_f32 v84, v84\n"
    "v_exp_f32 v85, v85\n"
    "v_exp_f32 v86, v86\n"
    "v_exp_f32 v87, v87\n"
    "v_exp_f32 v56, v56\n"
    "v_exp_f32 v57, v57\n"
    "v_smfmac_f32_32x32x32_bf16 v[104:119], v[252:255], v[80:87], v4\n"
    "v_exp_f32 v58, v58\n"
    "v_exp_f32 v59, v59\n"
    "v_exp_f32 v60, v60\n"
    "v_mfma_f32_32x32x16_bf16 v[72:87], v[244:247], v[36:39], v[40:55]\n"
    "v_exp_f32 v61, v61\n"
    "s_add_u32 s79, s77, s78\n"
    "s_add_u32 s78, s78, 4096\n"
    "buffer_load_dwordx4 v[244:247], v1, s[44:47], s79 offen offset:0 sc1\n"
    "s_and_b32 s78, s78, 32767\n"
    "v_exp_f32 v62, v62\n"
    "v_exp_f32 v63, v63\n"
    "v_exp_f32 v64, v64\n"
    "v_exp_f32 v65, v65\n"
    "v_smfmac_f32_32x32x32_bf16 v[120:135], v[248:251], v[56:63], v4\n"
    "v_exp_f32 v66, v66\n"
    "v_exp_f32 v67, v67\n"
    "buffer_load_dwordx4 v[176:179], v1, s[44:47], s98 offen offset:3072 sc1\n"
    "v_exp_f32 v68, v68\n"
    "v_exp_f32 v69, v69\n"
    "v_exp_f32 v70, v70\n"
    "v_exp_f32 v71, v71\n"
    "v_exp_f32 v72, v72\n"
    "v_exp_f32 v73, v73\n"
    "v_smfmac_f32_32x32x32_bf16 v[120:135], v[252:255], v[64:71], v4\n"
    "v_exp_f32 v74, v74\n"
    "v_exp_f32 v75, v75\n"
    "v_exp_f32 v76, v76\n"
    "s_waitcnt vmcnt(8)\n"
    "v_mfma_f32_32x32x16_bf16 v[56:71], v[232:235], v[24:27], v[40:55]\n"
    "v_exp_f32 v77, v77\n"
    "v_exp_f32 v78, v78\n"
    "v_exp_f32 v79, v79\n"
    "v_exp_f32 v80, v80\n"
    "v_exp_f32 v81, v81\n"
    "v_smfmac_f32_32x32x32_bf16 v[136:151], v[248:251], v[72:79], v4\n"
    "v_exp_f32 v82, v82\n"
    "v_exp_f32 v83, v83\n"
    "s_add_u32 s61, s98, 57344\n"
    "buffer_load_dwordx4 v[180:183], v1, s[44:47], s61 offen offset:3072 sc1\n"
    "v_exp_f32 v84, v84\n"
    "v_exp_f32 v85, v85\n"
    "v_exp_f32 v86, v86\n"
    "v_exp_f32 v87, v87\n"
    "v_exp_f32 v56, v56\n"
    "v_exp_f32 v57, v57\n"
    "v_smfmac_f32_32x32x32_bf16 v[136:151], v[252:255], v[80:87], v4\n"
    "v_exp_f32 v58, v58\n"
    "s_mov_b64 exec, s[34:35]\n"
    "buffer_load_dwordx4 v[248:251], v3, s[44:47], s79 offen offset:1024 sc1\n"
    "buffer_load_dwordx4 v[252:255], v3, s[44:47], s79 offen offset:1536 sc1\n"
    "s_mov_b64 exec, -1\n"
    "v_exp_f32 v59, v59\n"
    "v_exp_f32 v60, v60\n"
    "v_mfma_f32_32x32x16_bf16 v[72:87], v[232:235], v[28:31], v[40:55]\n"
    "v_exp_f32 v61, v61\n"
    "v_exp_f32 v62, v62\n"
    "v_exp_f32 v63, v63\n"
    "v_exp_f32 v64, v64\n"
    "v_exp_f32 v65, v65\n"
    "s_waitcnt vmcnt(7)\n"
    "v_smfmac_f32_32x32x32_bf16 v[88:103], v[236:239], v[56:63], v4\n"
    "v_exp_f32 v66, v66\n"
    "v_exp_f32 v67, v67\n"
    "buffer_load_dwordx4 v[184:187], v2, s[44:47], s98 offen offset:0 sc1\n"
    "v_exp_f32 v68, v68\n"
    "v_exp_f32 v69, v69\n"
    "v_exp_f32 v70, v70\n"
    "v_exp_f32 v71, v71\n"
    "v_exp_f32 v72, v72\n"
    "v_exp_f32 v73, v73\n"
    "v_smfmac_f32_32x32x32_bf16 v[88:103], v[240:243], v[64:71], v4\n"
    "v_exp_f32 v74, v74\n"
    "v_exp_f32 v75, v75\n"
    "v_exp_f32 v76, v76\n"
    "v_mfma_f32_32x32x16_bf16 v[56:71], v[232:235], v[32:35], v[40:55]\n"
    "v_exp_f32 v77, v77\n"
    "v_exp_f32 v78, v78\n"
    "v_exp_f32 v79, v79\n"
    "v_exp_f32 v80, v80\n"
    "v_exp_f32 v81, v81\n"
    "v_smfmac_f32_32x32x32_bf16 v[104:119], v[236:239], v[72:79], v4\n"
    "v_exp_f32 v82, v82\n"
    "v_exp_f32 v83, v83\n"
    "s_add_u32 s61, s98, 57344\n"
    "buffer_load_dwordx4 v[188:191], v2, s[44:47], s61 offen offset:0 sc1\n"
    "v_exp_f32 v84, v84\n"
    "v_exp_f32 v85, v85\n"
    "v_exp_f32 v86, v86\n"
    "v_exp_f32 v87, v87\n"
    "v_exp_f32 v56, v56\n"
    "v_exp_f32 v57, v57\n"
    "v_smfmac_f32_32x32x32_bf16 v[104:119], v[240:243], v[80:87], v4\n"
    "v_exp_f32 v58, v58\n"
    "v_exp_f32 v59, v59\n"
    "v_exp_f32 v60, v60\n"
    "v_mfma_f32_32x32x16_bf16 v[72:87], v[232:235], v[36:39], v[40:55]\n"
    "v_exp_f32 v61, v61\n"
    "buffer_load_dwordx4 v[232:235], v1, s[44:47], s79 offen offset:2048 sc1\n"
    "v_exp_f32 v62, v62\n"
    "v_exp_f32 v63, v63\n"
    "v_exp_f32 v64, v64\n"
    "v_exp_f32 v65, v65\n"
    "v_smfmac_f32_32x32x32_bf16 v[120:135], v[236:239], v[56:63], v4\n"
    "v_exp_f32 v66, v66\n"
    "v_exp_f32 v67, v67\n"
    "buffer_load_dwordx4 v[192:195], v2, s[44:47], s98 offen offset:1024 sc1\n"
    "v_exp_f32 v68, v68\n"
    "v_exp_f32 v69, v69\n"
    "v_exp_f32 v70, v70\n"
    "v_exp_f32 v71, v71\n"
    "v_exp_f32 v72, v72\n"
    "v_exp_f32 v73, v73\n"
    "v_smfmac_f32_32x32x32_bf16 v[120:135], v[240:243], v[64:71], v4\n"
    "v_exp_f32 v74, v74\n"
    "v_exp_f32 v75, v75\n"
    "v_exp_f32 v76, v76\n"
    "s_waitcnt vmcnt(8)\n"
    "v_mfma_f32_32x32x16_bf16 v[56:71], v[244:247], v[24:27], v[40:55]\n"
    "v_exp_f32 v77, v77\n"
    "v_exp_f32 v78, v78\n"
    "v_exp_f32 v79, v79\n"
    "v_exp_f32 v80, v80\n"
    "v_exp_f32 v81, v81\n"
    "v_smfmac_f32_32x32x32_bf16 v[136:151], v[236:239], v[72:79], v4\n"
    "v_exp_f32 v82, v82\n"
    "v_exp_f32 v83, v83\n"
    "s_add_u32 s61, s98, 57344\n"
    "buffer_load_dwordx4 v[196:199], v2, s[44:47], s61 offen offset:1024 sc1\n"
    "v_exp_f32 v84, v84\n"
    "v_exp_f32 v85, v85\n"
    "v_exp_f32 v86, v86\n"
    "v_exp_f32 v87, v87\n"
    "v_exp_f32 v56, v56\n"
    "v_exp_f32 v57, v57\n"
    "v_smfmac_f32_32x32x32_bf16 v[136:151], v[240:243], v[80:87], v4\n"
    "v_exp_f32 v58, v58\n"
    "s_mov_b64 exec, s[34:35]\n"
    "buffer_load_dwordx4 v[236:239], v3, s[44:47], s79 offen offset:3072 sc1\n"
    "buffer_load_dwordx4 v[240:243], v3, s[44:47], s79 offen offset:3584 sc1\n"
    "s_mov_b64 exec, -1\n"
    "v_exp_f32 v59, v59\n"
    "v_exp_f32 v60, v60\n"
    "v_mfma_f32_32x32x16_bf16 v[72:87], v[244:247], v[28:31], v[40:55]\n"
    "v_exp_f32 v61, v61\n"
    "v_exp_f32 v62, v62\n"
    "v_exp_f32 v63, v63\n"
    "v_exp_f32 v64, v64\n"
    "v_exp_f32 v65, v65\n"
    "s_waitcnt vmcnt(7)\n"
    "v_smfmac_f32_32x32x32_bf16 v[88:103], v[248:251], v[56:63], v4\n"
    "v_exp_f32 v66, v66\n"
    "v_exp_f32 v67, v67\n"
    "buffer_load_dwordx4 v[200:203], v2, s[44:47], s98 offen offset:2048 sc1\n"
    "v_exp_f32 v68, v68\n"
    "v_exp_f32 v69, v69\n"
    "v_exp_f32 v70, v70\n"
    "v_exp_f32 v71, v71\n"
    "v_exp_f32 v72, v72\n"
    "v_exp_f32 v73, v73\n"
    "v_smfmac_f32_32x32x32_bf16 v[88:103], v[252:255], v[64:71], v4\n"
    "v_exp_f32 v74, v74\n"
    "v_exp_f32 v75, v75\n"
    "v_exp_f32 v76, v76\n"
    "v_mfma_f32_32x32x16_bf16 v[56:71], v[244:247], v[32:35], v[40:55]\n"
    "v_exp_f32 v77, v77\n"
    "v_exp_f32 v78, v78\n"
    "v_exp_f32 v79, v79\n"
    "v_exp_f32 v80, v80\n"
    "v_exp_f32 v81, v81\n"
    "v_smfmac_f32_32x32x32_bf16 v[104:119], v[248:251], v[72:79], v4\n"
    "v_exp_f32 v82, v82\n"
    "v_exp_f32 v83, v83\n"
    "s_add_u32 s61, s98, 57344\n"
    "buffer_load_dwordx4 v[204:207], v2, s[44:47], s61 offen offset:2048 sc1\n"
    "v_exp_f32 v84, v84\n"
    "v_exp_f32 v85, v85\n"
    "v_exp_f32 v86, v86\n"
    "v_exp_f32 v87, v87\n"
    "v_exp_f32 v56, v56\n"
    "v_exp_f32 v57, v57\n"
    "v_smfmac_f32_32x32x32_bf16 v[104:119], v[252:255], v[80:87], v4\n"
    "v_exp_f32 v58, v58\n"
    "v_exp_f32 v59, v59\n"
    "v_exp_f32 v60, v60\n"
    "v_mfma_f32_32x32x16_bf16 v[72:87], v[244:247], v[36:39], v[40:55]\n"
    "v_exp_f32 v61, v61\n"
    "s_add_u32 s79, s77, s78\n"
    "s_add_u32 s78, s78, 4096\n"
    "buffer_load_dwordx4 v[244:247], v1, s[44:47], s79 offen offset:0 sc1\n"
    "s_and_b32 s78, s78, 32767\n"
    "v_exp_f32 v62, v62\n"
    "v_exp_f32 v63, v63\n"
    "v_exp_f32 v64, v64\n"
    "v_exp_f32 v65, v65\n"
    "v_smfmac_f32_32x32x32_bf16 v[120:135], v[248:251], v[56:63], v4\n"
    "v_exp_f32 v66, v66\n"
    "v_exp_f32 v67, v67\n"
    "buffer_load_dwordx4 v[208:211], v1, s[44:47], s99 offen offset:0 sc1\n"
    "v_exp_f32 v68, v68\n"
    "v_exp_f32 v69, v69\n"
    "v_exp_f32 v70, v70\n"
    "v_exp_f32 v71, v71\n"
    "v_exp_f32 v72, v72\n"
    "v_exp_f32 v73, v73\n"
    "v_smfmac_f32_32x32x32_bf16 v[120:135], v[252:255], v[64:71], v4\n"
    "v_exp_f32 v74, v74\n"
    "v_exp_f32 v75, v75\n"
    "v_exp_f32 v76, v76\n"
    "s_waitcnt vmcnt(8)\n"
    "v_mfma_f32_32x32x16_bf16 v[56:71], v[232:235], v[24:27], v[40:55]\n"
    "v_exp_f32 v77, v77\n"
    "v_exp_f32 v78, v78\n"
    "v_exp_f32 v79, v79\n"
    "v_exp_f32 v80, v80\n"
    "v_exp_f32 v81, v81\n"
    "v_smfmac_f32_32x32x32_bf16 v[136:151], v[248:251], v[72:79], v4\n"
    "v_exp_f32 v82, v82\n"
    "v_exp_f32 v83, v83\n"
    "s_add_u32 s61, s99, 57344\n"
    "buffer_load_dwordx4 v[212:215], v1, s[44:47], s61 offen offset:0 sc1\n"
    "v_exp_f32 v84, v84\n"
    "v_exp_f32 v85, v85\n"
    "v_exp_f32 v86, v86\n"
    "v_exp_f32 v87, v87\n"
    "v_exp_f32 v56, v56\n"
    "v_exp_f32 v57, v57\n"
    "v_smfmac_f32_32x32x32_bf16 v[136:151], v[252:255], v[80:87], v4\n"
    "v_exp_f32 v58, v58\n"
    "s_mov_b64 exec, s[34:35]\n"
    "buffer_load_dwordx4 v[248:251], v3, s[44:47], s79 offen offset:1024 sc1\n"
    "buffer_load_dwordx4 v[252:255], v3, s[44:47], s79 offen offset:1536 sc1\n"
    "s_mov_b64 exec, -1\n"
    "v_exp_f32 v59, v59\n"
    "v_exp_f32 v60, v60\n"
    "v_mfma_f32_32x32x16_bf16 v[72:87], v[232:235], v[28:31], v[40:55]\n"
    "v_exp_f32 v61, v61\n"
    "v_exp_f32 v62, v62\n"
    "v_exp_f32 v63, v63\n"
    "v_exp_f32 v64, v64\n"
    "v_exp_f32 v65, v65\n"
    "s_waitcnt vmcnt(7)\n"
    "v_smfmac_f32_32x32x32_bf16 v[88:103], v[236:239], v[56:63], v4\n"
    "v_exp_f32 v66, v66\n"
    "v_exp_f32 v67, v67\n"
    "buffer_load_dwordx4 v[216:219], v1, s[44:47], s99 offen offset:1024 sc1\n"
    "v_exp_f32 v68, v68\n"
    "v_exp_f32 v69, v69\n"
    "v_exp_f32 v70, v70\n"
    "v_exp_f32 v71, v71\n"
    "v_exp_f32 v72, v72\n"
    "v_exp_f32 v73, v73\n"
    "v_smfmac_f32_32x32x32_bf16 v[88:103], v[240:243], v[64:71], v4\n"
    "v_exp_f32 v74, v74\n"
    "v_exp_f32 v75, v75\n"
    "v_exp_f32 v76, v76\n"
    "v_mfma_f32_32x32x16_bf16 v[56:71], v[232:235], v[32:35], v[40:55]\n"
    "v_exp_f32 v77, v77\n"
    "v_exp_f32 v78, v78\n"
    "v_exp_f32 v79, v79\n"
    "v_exp_f32 v80, v80\n"
    "v_exp_f32 v81, v81\n"
    "v_smfmac_f32_32x32x32_bf16 v[104:119], v[236:239], v[72:79], v4\n"
    "v_exp_f32 v82, v82\n"
    "v_exp_f32 v83, v83\n"
    "s_add_u32 s61, s99, 57344\n"
    "buffer_load_dwordx4 v[220:223], v1, s[44:47], s61 offen offset:1024 sc1\n"
    "v_exp_f32 v84, v84\n"
    "v_exp_f32 v85, v85\n"
    "v_exp_f32 v86, v86\n"
    "v_exp_f32 v87, v87\n"
    "v_exp_f32 v56, v56\n"
    "v_exp_f32 v57, v57\n"
    "v_smfmac_f32_32x32x32_bf16 v[104:119], v[240:243], v[80:87], v4\n"
    "v_exp_f32 v58, v58\n"
    "v_exp_f32 v59, v59\n"
    "v_exp_f32 v60, v60\n"
    "v_mfma_f32_32x32x16_bf16 v[72:87], v[232:235], v[36:39], v[40:55]\n"
    "v_exp_f32 v61, v61\n"
    "buffer_load_dwordx4 v[232:235], v1, s[44:47], s79 offen offset:2048 sc1\n"
    "v_exp_f32 v62, v62\n"
    "v_exp_f32 v63, v63\n"
    "v_exp_f32 v64, v64\n"
    "v_exp_f32 v65, v65\n"
    "v_smfmac_f32_32x32x32_bf16 v[120:135], v[236:239], v[56:63], v4\n"
    "v_exp_f32 v66, v66\n"
    "v_exp_f32 v67, v67\n"
    "buffer_load_dwordx4 v[224:227], v1, s[44:47], s99 offen offset:2048 sc1\n"
    "v_exp_f32 v68, v68\n"
    "v_exp_f32 v69, v69\n"
    "v_exp_f32 v70, v70\n"
    "v_exp_f32 v71, v71\n"
    "v_exp_f32 v72, v72\n"
    "v_exp_f32 v73, v73\n"
    "v_smfmac_f32_32x32x32_bf16 v[120:135], v[240:243], v[64:71], v4\n"
    "v_exp_f32 v74, v74\n"
    "v_exp_f32 v75, v75\n"
    "v_exp_f32 v76, v76\n"
    "s_waitcnt vmcnt(8)\n"
    "v_mfma_f32_32x32x16_bf16 v[56:71], v[244:247], v[24:27], v[40:55]\n"
    "v_exp_f32 v77, v77\n"
    "v_exp_f32 v78, v78\n"
    "v_exp_f32 v79, v79\n"
    "v_exp_f32 v80, v80\n"
    "v_exp_f32 v81, v81\n"
    "v_smfmac_f32_32x32x32_bf16 v[136:151], v[236:239], v[72:79], v4\n"
    "v_exp_f32 v82, v82\n"
    "v_exp_f32 v83, v83\n"
    "s_add_u32 s61, s99, 57344\n"
    "buffer_load_dwordx4 v[228:231], v1, s[44:47], s61 offen offset:2048 sc1\n"
    "v_exp_f32 v84, v84\n"
    "v_exp_f32 v85, v85\n"
    "v_exp_f32 v86, v86\n"
    "v_exp_f32 v87, v87\n"
    "v_exp_f32 v56, v56\n"
    "v_exp_f32 v57, v57\n"
    "v_smfmac_f32_32x32x32_bf16 v[136:151], v[240:243], v[80:87], v4\n"
    "v_exp_f32 v58, v58\n"
    "s_mov_b64 exec, s[34:35]\n"
    "buffer_load_dwordx4 v[236:239], v3, s[44:47], s79 offen offset:3072 sc1\n"
    "buffer_load_dwordx4 v[240:243], v3, s[44:47], s79 offen offset:3584 sc1\n"
    "s_mov_b64 exec, -1\n"
    "v_exp_f32 v59, v59\n"
    "v_exp_f32 v60, v60\n"
    "v_mfma_f32_32x32x16_bf16 v[72:87], v[244:247], v[28:31], v[40:55]\n"
    "v_exp_f32 v61, v61\n"
    "v_exp_f32 v62, v62\n"
    "v_exp_f32 v63, v63\n"
    "v_exp_f32 v64, v64\n"
    "v_exp_f32 v65, v65\n"
    "s_waitcnt vmcnt(7)\n"
    "v_smfmac_f32_32x32x32_bf16 v[88:103], v[248:251], v[56:63], v4\n"
    "v_exp_f32 v66, v66\n"
    "v_exp_f32 v67, v67\n"
    "v_exp_f32 v68, v68\n"
    "v_exp_f32 v69, v69\n"
    "v_exp_f32 v70, v70\n"
    "v_exp_f32 v71, v71\n"
    "v_exp_f32 v72, v72\n"
    "v_exp_f32 v73, v73\n"
    "v_smfmac_f32_32x32x32_bf16 v[88:103], v[252:255], v[64:71], v4\n"
    "v_exp_f32 v74, v74\n"
    "v_exp_f32 v75, v75\n"
    "v_exp_f32 v76, v76\n"
    "v_mfma_f32_32x32x16_bf16 v[56:71], v[244:247], v[32:35], v[40:55]\n"
    "v_exp_f32 v77, v77\n"
    "v_exp_f32 v78, v78\n"
    "v_exp_f32 v79, v79\n"
    "v_exp_f32 v80, v80\n"
    "v_exp_f32 v81, v81\n"
    "v_smfmac_f32_32x32x32_bf16 v[104:119], v[248:251], v[72:79], v4\n"
    "v_exp_f32 v82, v82\n"
    "v_exp_f32 v83, v83\n"
    "v_and_b32 v10, 15, v7\n"
    "v_lshlrev_b32 v10, 10, v10\n"
    "buffer_load_dword v11, v10, s[28:31], s85 offen\n"
    "v_exp_f32 v84, v84\n"
    "v_exp_f32 v85, v85\n"
    "v_exp_f32 v86, v86\n"
    "v_exp_f32 v87, v87\n"
    "v_exp_f32 v56, v56\n"
    "v_exp_f32 v57, v57\n"
    "v_smfmac_f32_32x32x32_bf16 v[104:119], v[252:255], v[80:87], v4\n"
    "v_exp_f32 v58, v58\n"
    "v_exp_f32 v59, v59\n"
    "v_exp_f32 v60, v60\n"
    "v_mfma_f32_32x32x16_bf16 v[72:87], v[244:247], v[36:39], v[40:55]\n"
    "v_exp_f32 v61, v61\n"
    "s_add_u32 s79, s77, s78\n"
    "s_add_u32 s78, s78, 4096\n"
    "buffer_load_dwordx4 v[244:247], v1, s[44:47], s79 offen offset:0 sc1\n"
    "s_and_b32 s78, s78, 32767\n"
    "v_exp_f32 v62, v62\n"
    "v_exp_f32 v63, v63\n"
    "v_exp_f32 v64, v64\n"
    "v_exp_f32 v65, v65\n"
    "v_smfmac_f32_32x32x32_bf16 v[120:135], v[248:251], v[56:63], v4\n"
    "v_exp_f32 v66, v66\n"
    "v_exp_f32 v67, v67\n"
    "v_min_u32 v10, 27, v7\n"
    "v_add_u32 v10, 0, v10\n"
    "v_lshlrev_b32 v10, 10, v10\n"
    "buffer_load_dword v11, v10, s[24:27], s86 offen\n"
    "v_exp_f32 v68, v68\n"
    "v_exp_f32 v69, v69\n"
    "v_exp_f32 v70, v70\n"
    "v_exp_f32 v71, v71\n"
    "v_exp_f32 v72, v72\n"
    "v_exp_f32 v73, v73\n"
    "v_smfmac_f32_32x32x32_bf16 v[120:135], v[252:255], v[64:71], v4\n"
    "v_exp_f32 v74, v74\n"
    "v_exp_f32 v75, v75\n"
    "v_exp_f32 v76, v76\n"
    "s_waitcnt vmcnt(7)\n"
    "v_mfma_f32_32x32x16_bf16 v[56:71], v[232:235], v[24:27], v[40:55]\n"
    "v_exp_f32 v77, v77\n"
    "v_exp_f32 v78, v78\n"
    "v_exp_f32 v79, v79\n"
    "v_exp_f32 v80, v80\n"
    "v_exp_f32 v81, v81\n"
    "v_smfmac_f32_32x32x32_bf16 v[136:151], v[248:251], v[72:79], v4\n"
    "v_exp_f32 v82, v82\n"
    "v_exp_f32 v83, v83\n"
    "v_min_u32 v10, 27, v7\n"
    "v_add_u32 v10, 28, v10\n"
    "v_lshlrev_b32 v10, 10, v10\n"
    "buffer_load_dword v11, v10, s[24:27], s86 offen\n"
    "v_exp_f32 v84, v84\n"
    "v_exp_f32 v85, v85\n"
    "v_exp_f32 v86, v86\n"
    "v_exp_f32 v87, v87\n"
    "v_exp_f32 v56, v56\n"
    "v_exp_f32 v57, v57\n"
    "v_smfmac_f32_32x32x32_bf16 v[136:151], v[252:255], v[80:87], v4\n"
    "v_exp_f32 v58, v58\n"
    "s_mov_b64 exec, s[34:35]\n"
    "buffer_load_dwordx4 v[248:251], v3, s[44:47], s79 offen offset:1024 sc1\n"
    "buffer_load_dwordx4 v[252:255], v3, s[44:47], s79 offen offset:1536 sc1\n"
    "s_mov_b64 exec, -1\n"
    "v_exp_f32 v59, v59\n"
    "v_exp_f32 v60, v60\n"
    "v_mfma_f32_32x32x16_bf16 v[72:87], v[232:235], v[28:31], v[40:55]\n"
    "v_exp_f32 v61, v61\n"
    "v_exp_f32 v62, v62\n"
    "v_exp_f32 v63, v63\n"
    "v_exp_f32 v64, v64\n"
    "v_exp_f32 v65, v65\n"
    "s_waitcnt vmcnt(6)\n"
    "v_smfmac_f32_32x32x32_bf16 v[88:103], v[236:239], v[56:63], v4\n"
    "v_exp_f32 v66, v66\n"
    "v_exp_f32 v67, v67\n"
    "v_exp_f32 v68, v68\n"
    "v_exp_f32 v69, v69\n"
    "v_exp_f32 v70, v70\n"
    "v_exp_f32 v71, v71\n"
    "v_exp_f32 v72, v72\n"
    "v_exp_f32 v73, v73\n"
    "v_smfmac_f32_32x32x32_bf16 v[88:103], v[240:243], v[64:71], v4\n"
    "v_exp_f32 v74, v74\n"
    "v_exp_f32 v75, v75\n"
    "v_exp_f32 v76, v76\n"
    "v_mfma_f32_32x32x16_bf16 v[56:71], v[232:235], v[32:35], v[40:55]\n"
    "v_exp_f32 v77, v77\n"
    "v_exp_f32 v78, v78\n"
    "v_exp_f32 v79, v79\n"
    "v_exp_f32 v80, v80\n"
    "v_exp_f32 v81, v81\n"
    "v_smfmac_f32_32x32x32_bf16 v[104:119], v[236:239], v[72:79], v4\n"
    "v_exp_f32 v82, v82\n"
    "v_exp_f32 v83, v83\n"
    "v_exp_f32 v84, v84\n"
    "v_exp_f32 v85, v85\n"
    "v_exp_f32 v86, v86\n"
    "v_exp_f32 v87, v87\n"
    "v_exp_f32 v56, v56\n"
    "v_exp_f32 v57, v57\n"
    "v_smfmac_f32_32x32x32_bf16 v[104:119], v[240:243], v[80:87], v4\n"
    "v_exp_f32 v58, v58\n"
    "v_exp_f32 v59, v59\n"
    "v_exp_f32 v60, v60\n"
    "v_mfma_f32_32x32x16_bf16 v[72:87], v[232:235], v[36:39], v[40:55]\n"
    "v_exp_f32 v61, v61\n"
    "buffer_load_dwordx4 v[232:235], v1, s[44:47], s79 offen offset:2048 sc1\n"
    "v_exp_f32 v62, v62\n"
    "v_exp_f32 v63, v63\n"
    "v_exp_f32 v64, v64\n"
    "v_exp_f32 v65, v65\n"
    "v_smfmac_f32_32x32x32_bf16 v[120:135], v[236:239], v[56:63], v4\n"
    "v_exp_f32 v66, v66\n"
    "v_exp_f32 v67, v67\n"
    "v_exp_f32 v68, v68\n"
    "v_exp_f32 v69, v69\n"
    "v_exp_f32 v70, v70\n"
    "v_exp_f32 v71, v71\n"
    "v_exp_f32 v72, v72\n"
    "v_exp_f32 v73, v73\n"
    "v_smfmac_f32_32x32x32_bf16 v[120:135], v[240:243], v[64:71], v4\n"
    "v_exp_f32 v74, v74\n"
    "v_exp_f32 v75, v75\n"
    "v_exp_f32 v76, v76\n"
    "s_waitcnt vmcnt(5)\n"
    "v_mfma_f32_32x32x16_bf16 v[56:71], v[244:247], v[24:27], v[40:55]\n"
    "v_exp_f32 v77, v77\n"
    "v_exp_f32 v78, v78\n"
    "v_exp_f32 v79, v79\n"
    "v_exp_f32 v80, v80\n"
    "v_exp_f32 v81, v81\n"
    "v_smfmac_f32_32x32x32_bf16 v[136:151], v[236:239], v[72:79], v4\n"
    "v_exp_f32 v82, v82\n"
    "v_exp_f32 v83, v83\n"
    "v_exp_f32 v84, v84\n"
    "v_exp_f32 v85, v85\n"
    "v_exp_f32 v86, v86\n"
    "v_exp_f32 v87, v87\n"
    "s_setprio 0\n"
    "v_exp_f32 v56, v56\n"
    "v_exp_f32 v57, v57\n"
    "v_smfmac_f32_32x32x32_bf16 v[136:151], v[240:243], v[80:87], v4\n"
    "v_exp_f32 v58, v58\n"
    "s_mov_b64 exec, s[34:35]\n"
    "buffer_load_dwordx4 v[236:239], v3, s[44:47], s79 offen offset:3072 sc1\n"
    "buffer_load_dwordx4 v[240:243], v3, s[44:47], s79 offen offset:3584 sc1\n"
    "s_mov_b64 exec, -1\n"
    "v_exp_f32 v59, v59\n"
    "v_exp_f32 v60, v60\n"
    "v_mfma_f32_32x32x16_bf16 v[72:87], v[244:247], v[28:31], v[40:55]\n"
    "v_exp_f32 v61, v61\n"
    "v_exp_f32 v62, v62\n"
    "v_exp_f32 v63, v63\n"
    "v_exp_f32 v64, v64\n"
    "v_exp_f32 v65, v65\n"
    "s_waitcnt vmcnt(3)\n"
    "v_smfmac_f32_32x32x32_bf16 v[88:103], v[248:251], v[56:63], v4\n"
    "v_exp_f32 v66, v66\n"
    "v_exp_f32 v67, v67\n"
    "v_exp_f32 v68, v68\n"
    "v_exp_f32 v69, v69\n"
    "v_exp_f32 v70, v70\n"
    "v_exp_f32 v71, v71\n"
    "v_exp_f32 v72, v72\n"
    "v_exp_f32 v73, v73\n"
    "v_smfmac_f32_32x32x32_bf16 v[88:103], v[252:255], v[64:71], v4\n"
    "v_exp_f32 v74, v74\n"
    "v_exp_f32 v75, v75\n"
    "v_exp_f32 v76, v76\n"
    "v_mfma_f32_32x32x16_bf16 v[56:71], v[244:247], v[32:35], v[40:55]\n"
    "v_exp_f32 v77, v77\n"
    "v_exp_f32 v78, v78\n"
    "v_exp_f32 v79, v79\n"
    "v_exp_f32 v80, v80\n"
    "v_exp_f32 v81, v81\n"
    "v_smfmac_f32_32x32x32_bf16 v[104:119], v[248:251], v[72:79], v4\n"
    "v_exp_f32 v82, v82\n"
    "v_exp_f32 v83, v83\n"
    "v_exp_f32 v84, v84\n"
    "v_exp_f32 v85, v85\n"
    "v_exp_f32 v86, v86\n"
    "v_exp_f32 v87, v87\n"
    "v_exp_f32 v56, v56\n"
    "v_exp_f32 v57, v57\n"
    "v_smfmac_f32_32x32x32_bf16 v[104:119], v[252:255], v[80:87], v4\n"
    "v_exp_f32 v58, v58\n"
    "v_exp_f32 v59, v59\n"
    "v_exp_f32 v60, v60\n"
    "v_mfma_f32_32x32x16_bf16 v[72:87], v[244:247], v[36:39], v[40:55]\n"
    "v_exp_f32 v61, v61\n"
    "v_exp_f32 v62, v62\n"
    "v_exp_f32 v63, v63\n"
    "v_exp_f32 v64, v64\n"
    "v_exp_f32 v65, v65\n"
    "v_smfmac_f32_32x32x32_bf16 v[120:135], v[248:251], v[56:63], v4\n"
    "v_exp_f32 v66, v66\n"
    "v_exp_f32 v67, v67\n"
    "v_exp_f32 v68, v68\n"
    "v_exp_f32 v69, v69\n"
    "v_exp_f32 v70, v70\n"
    "v_exp_f32 v71, v71\n"
    "v_exp_f32 v72, v72\n"
    "v_exp_f32 v73, v73\n"
    "v_smfmac_f32_32x32x32_bf16 v[120:135], v[252:255], v[64:71], v4\n"
    "v_exp_f32 v74, v74\n"
    "v_exp_f32 v75, v75\n"
    "v_exp_f32 v76, v76\n"
    "s_waitcnt vmcnt(2)\n"
    "v_mfma_f32_32x32x16_bf16 v[56:71], v[232:235], v[24:27], v[40:55]\n"
    "v_exp_f32 v77, v77\n"
    "v_exp_f32 v78, v78\n"
    "v_exp_f32 v79, v79\n"
    "v_exp_f32 v80, v80\n"
    "v_exp_f32 v81, v81\n"
    "v_smfmac_f32_32x32x32_bf16 v[136:151], v[248:251], v[72:79], v4\n"
    "v_exp_f32 v82, v82\n"
    "v_exp_f32 v83, v83\n"
    "buffer_load_dwordx4 v[24:27], v1, s[44:47], s99 offen offset:3072 sc1\n"
    "v_exp_f32 v84, v84\n"
    "v_exp_f32 v85, v85\n"
    "v_exp_f32 v86, v86\n"
    "v_exp_f32 v87, v87\n"
    "v_exp_f32 v56, v56\n"
    "v_exp_f32 v57, v57\n"
    "v_smfmac_f32_32x32x32_bf16 v[136:151], v[252:255], v[80:87], v4\n"
    "v_exp_f32 v58, v58\n"
    "v_exp_f32 v59, v59\n"
    "v_exp_f32 v60, v60\n"
    "v_mfma_f32_32x32x16_bf16 v[72:87], v[232:235], v[28:31], v[40:55]\n"
    "v_exp_f32 v61, v61\n"
    "v_exp_f32 v62, v62\n"
    "v_exp_f32 v63, v63\n"
    "v_exp_f32 v64, v64\n"
    "v_exp_f32 v65, v65\n"
    "s_waitcnt vmcnt(1)\n"
    "v_smfmac_f32_32x32x32_bf16 v[88:103], v[236:239], v[56:63], v4\n"
    "v_exp_f32 v66, v66\n"
    "v_exp_f32 v67, v67\n"
    "s_add_u32 s61, s99, 57344\n"
    "buffer_load_dwordx4 v[28:31], v1, s[44:47], s61 offen offset:3072 sc1\n"
    "v_exp_f32 v68, v68\n"
    "v_exp_f32 v69, v69\n"
    "v_exp_f32 v70, v70\n"
    "v_exp_f32 v71, v71\n"
    "v_exp_f32 v72, v72\n"
    "v_exp_f32 v73, v73\n"
    "v_smfmac_f32_32x32x32_bf16 v[88:103], v[240:243], v[64:71], v4\n"
    "v_exp_f32 v74, v74\n"
    "v_exp_f32 v75, v75\n"
    "v_exp_f32 v76, v76\n"
    "v_mfma_f32_32x32x16_bf16 v[56:71], v[232:235], v[32:35], v[40:55]\n"
    "v_exp_f32 v77, v77\n"
    "v_exp_f32 v78, v78\n"
    "v_exp_f32 v79, v79\n"
    "v_exp_f32 v80, v80\n"
    "v_exp_f32 v81, v81\n"
    "v_smfmac_f32_32x32x32_bf16 v[104:119], v[236:239], v[72:79], v4\n"
    "v_exp_f32 v82, v82\n"
    "v_exp_f32 v83, v83\n"
    "buffer_load_dwordx4 v[32:35], v2, s[44:47], s99 offen offset:0 sc1\n"
    "v_exp_f32 v84, v84\n"
    "v_exp_f32 v85, v85\n"
    "v_exp_f32 v86, v86\n"
    "v_exp_f32 v87, v87\n"
    "v_exp_f32 v56, v56\n"
    "v_exp_f32 v57, v57\n"
    "v_smfmac_f32_32x32x32_bf16 v[104:119], v[240:243], v[80:87], v4\n"
    "v_exp_f32 v58, v58\n"
    "v_exp_f32 v59, v59\n"
    "v_exp_f32 v60, v60\n"
    "v_mfma_f32_32x32x16_bf16 v[72:87], v[232:235], v[36:39], v[40:55]\n"
    "v_exp_f32 v61, v61\n"
    "v_exp_f32 v62, v62\n"
    "v_exp_f32 v63, v63\n"
    "s_add_u32 s61, s99, 57344\n"
    "buffer_load_dwordx4 v[36:39], v2, s[44:47], s61 offen offset:0 sc1\n"
    "v_exp_f32 v64, v64\n"
    "v_exp_f32 v65, v65\n"
    "v_smfmac_f32_32x32x32_bf16 v[120:135], v[236:239], v[56:63], v4\n"
    "v_exp_f32 v66, v66\n"
    "v_exp_f32 v67, v67\n"
    "buffer_load_dwordx4 v[40:43], v2, s[44:47], s99 offen offset:1024 sc1\n"
    "v_exp_f32 v68, v68\n"
    "v_exp_f32 v69, v69\n"
    "v_exp_f32 v70, v70\n"
    "v_exp_f32 v71, v71\n"
    "v_exp_f32 v72, v72\n"
    "v_exp_f32 v73, v73\n"
    "v_smfmac_f32_32x32x32_bf16 v[120:135], v[240:243], v[64:71], v4\n"
    "v_exp_f32 v74, v74\n"
    "v_exp_f32 v75, v75\n"
    "s_add_u32 s61, s99, 57344\n"
    "buffer_load_dwordx4 v[44:47], v2, s[44:47], s61 offen offset:1024 sc1\n"
    "v_exp_f32 v76, v76\n"
    "v_exp_f32 v77, v77\n"
    "v_exp_f32 v78, v78\n"
    "v_exp_f32 v79, v79\n"
    "buffer_load_dwordx4 v[48:51], v2, s[44:47], s99 offen offset:2048 sc1\n"
    "v_exp_f32 v80, v80\n"
    "v_exp_f32 v81, v81\n"
    "v_smfmac_f32_32x32x32_bf16 v[136:151], v[236:239], v[72:79], v4\n"
    "v_exp_f32 v82, v82\n"
    "v_exp_f32 v83, v83\n"
    "s_add_u32 s61, s99, 57344\n"
    "buffer_load_dwordx4 v[52:55], v2, s[44:47], s61 offen offset:2048 sc1\n"
    "v_exp_f32 v84, v84\n"
    "v_exp_f32 v85, v85\n"
    "v_exp_f32 v86, v86\n"
    "v_exp_f32 v87, v87\n"
    "s_nop 1\n"
    "v_smfmac_f32_32x32x32_bf16 v[136:151], v[240:243], v[80:87], v4\n"
    "s_mul_i32 s61, s16, 27648\n"
    "v_add_u32 v10, s61, v1\n"
    "v_lshlrev_b32 v12, 2, v7\n"
    "v_add_u32 v11, s61, v12\n"
    "s_mul_i32 s62, s80, 6912\n"
    "s_nop 3\n"
    "s_cmp_ge_u32 s80, 2\n"
    "s_cbranch_scc1 Lq_27_%=\n"
    "s_cmp_eq_u32 s80, 1\n"
    "s_cbranch_scc1 Lq_26_%=\n"
    "ds_write_b128 v10, v[104:107] offset:6912\n"
    "ds_write_b128 v10, v[108:111] offset:7936\n"
    "ds_write_b32 v11, v112 offset:8960\n"
    "ds_write_b128 v10, v[120:123] offset:16128\n"
    "ds_write_b128 v10, v[124:127] offset:17152\n"
    "ds_write_b32 v11, v128 offset:18176\n"
    "ds_write_b128 v10, v[136:139] offset:25344\n"
    "ds_write_b128 v10, v[140:143] offset:26368\n"
    "ds_write_b32 v11, v144 offset:27392\n"
    "v_mov_b64 v[236:237], v[88:89]\n"
    "v_mov_b64 v[238:239], v[90:91]\n"
    "v_mov_b64 v[240:241], v[92:93]\n"
    "v_mov_b64 v[242:243], v[94:95]\n"
    "v_mov_b32 v232, v96\n"
    "s_branch Lqj_29_%=\n"
    "Lq_26_%=:\n"
    "ds_write_b128 v10, v[120:123] offset:18432\n"
    "ds_write_b128 v10, v[124:127] offset:19456\n"
    "ds_write_b32 v11, v128 offset:20480\n"
    "ds_write_b128 v10, v[136:139] offset:23040\n"
    "ds_write_b128 v10, v[140:143] offset:24064\n"
    "ds_write_b32 v11, v144 offset:25088\n"
    "ds_write_b128 v10, v[88:91] offset:0\n"
    "ds_write_b128 v10, v[92:95] offset:1024\n"
    "ds_write_b32 v11, v96 offset:2048\n"
    "v_mov_b64 v[236:237], v[104:105]\n"
    "v_mov_b64 v[238:239], v[106:107]\n"
    "v_mov_b64 v[240:241], v[108:109]\n"
    "v_mov_b64 v[242:243], v[110:111]\n"
    "v_mov_b32 v232, v112\n"
    "s_branch Lqj_29_%=\n"
    "Lq_27_%=:\n"
    "s_cmp_eq_u32 s80, 3\n"
    "s_cbranch_scc1 Lq_28_%=\n"
    "ds_write_b128 v10, v[88:91] offset:2304\n"
    "ds_write_b128 v10, v[92:95] offset:3328\n"
    "ds_write_b32 v11, v96 offset:4352\n"
    "ds_write_b128 v10, v[104:107] offset:11520\n"
    "ds_write_b128 v10, v[108:111] offset:12544\n"
    "ds_write_b32 v11, v112 offset:13568\n"
    "ds_write_b128 v10, v[136:139] offset:20736\n"
    "ds_write_b128 v10, v[140:143] offset:21760\n"
    "ds_write_b32 v11, v144 offset:22784\n"
    "v_mov_b64 v[236:237], v[120:121]\n"
    "v_mov_b64 v[238:239], v[122:123]\n"
    "v_mov_b64 v[240:241], v[124:125]\n"
    "v_mov_b64 v[242:243], v[126:127]\n"
    "v_mov_b32 v232, v128\n"
    "s_branch Lqj_29_%=\n"
    "Lq_28_%=:\n"
    "ds_write_b128 v10, v[88:91] offset:4608\n"
    "ds_write_b128 v10, v[92:95] offset:5632\n"
    "ds_write_b32 v11, v96 offset:6656\n"
    "ds_write_b128 v10, v[104:107] offset:9216\n"
    "ds_write_b128 v10, v[108:111] offset:10240\n"
    "ds_write_b32 v11, v112 offset:11264\n"
    "ds_write_b128 v10, v[120:123] offset:13824\n"
    "ds_write_b128 v10, v[124:127] offset:14848\n"
    "ds_write_b32 v11, v128 offset:15872\n"
    "v_mov_b64 v[236:237], v[136:137]\n"
    "v_mov_b64 v[238:239], v[138:139]\n"
    "v_mov_b64 v[240:241], v[140:141]\n"
    "v_mov_b64 v[242:243], v[142:143]\n"
    "v_mov_b32 v232, v144\n"
    "Lqj_29_%=:\n"
    "v_add_u32 v10, s62, v10\n"
    "v_add_u32 v11, s62, v11\n"
    "s_waitcnt lgkmcnt(0)\n"
    "s_barrier\n"
    "ds_read_b128 v[120:123], v10 offset:0\n"
    "ds_read_b128 v[124:127], v10 offset:1024\n"
    "ds_read_b32 v128, v11 offset:2048\n"
    "ds_read_b128 v[130:133], v10 offset:2304\n"
    "ds_read_b128 v[134:137], v10 offset:3328\n"
    "ds_read_b32 v138, v11 offset:4352\n"
    "ds_read_b128 v[140:143], v10 offset:4608\n"
    "ds_read_b128 v[144:147], v10 offset:5632\n"
    "ds_read_b32 v148, v11 offset:6656\n"
    "ds_read_b64_tr_b16 v[56:57], v18 offset:0\n"
    "ds_read_b64_tr_b16 v[58:59], v18 offset:2048\n"
    "ds_read_b64_tr_b16 v[60:61], v19 offset:0\n"
    "ds_read_b64_tr_b16 v[62:63], v19 offset:2048\n"
    "ds_read_b64_tr_b16 v[64:65], v20 offset:0\n"
    "ds_read_b64_tr_b16 v[66:67], v20 offset:2048\n"
    "v_mov_b64 v[12:13], 0\n"
    "s_waitcnt lgkmcnt(12)\n"
    "ds_read_b64_tr_b16 v[68:69], v21 offset:0\n"
    "ds_read_b64_tr_b16 v[70:71], v21 offset:2048\n"
    "v_pk_add_f32 v[236:237], v[236:237], v[120:121]\n"
    "v_pk_add_f32 v[238:239], v[238:239], v[122:123]\n"
    "v_pk_add_f32 v[240:241], v[240:241], v[124:125]\n"
    "v_pk_add_f32 v[242:243], v[242:243], v[126:127]\n"
    "v_add_f32 v232, v232, v128\n"
    "s_waitcnt lgkmcnt(11)\n"
    "v_pk_add_f32 v[236:237], v[236:237], v[130:131]\n"
    "v_pk_add_f32 v[238:239], v[238:239], v[132:133]\n"
    "v_pk_add_f32 v[240:241], v[240:241], v[134:135]\n"
    "v_pk_add_f32 v[242:243], v[242:243], v[136:137]\n"
    "v_add_f32 v232, v232, v138\n"
    "s_waitcnt lgkmcnt(8)\n"
    "v_pk_add_f32 v[236:237], v[236:237], v[140:141]\n"
    "v_pk_add_f32 v[238:239], v[238:239], v[142:143]\n"
    "v_pk_add_f32 v[240:241], v[240:241], v[144:145]\n"
    "v_pk_add_f32 v[242:243], v[242:243], v[146:147]\n"
    "v_add_f32 v232, v232, v148\n"
    "v_pk_mul_f32 v[10:11], v[236:237], v[12:13]\n"
    "v_pk_fma_f32 v[10:11], v[238:239], v[12:13], v[10:11]\n"
    "v_pk_fma_f32 v[10:11], v[240:241], v[12:13], v[10:11]\n"
    "v_pk_fma_f32 v[10:11], v[242:243], v[12:13], v[10:11]\n"
    "v_add_f32 v10, v10, v11\n"
    "v_mov_b32 v11, 0x71800000\n"
    "v_add_f32 v10, v10, v232\n"
    "v_cmp_nlt_f32 vcc, 0x1f800000, v10\n"
    "v_cmp_ngt_f32 s[48:49], v11, v10\n"
    "s_nop 1\n"
    "s_or_b64 vcc, vcc, s[48:49]\n"
    "s_cbranch_vccnz Lfallback_30_%=\n"
    "Lok_31_%=:\n"
    "v_rcp_f32 v232, v232\n"
    "s_nop 0\n"
    "v_mul_f32 v232, v232, v23\n"
    "v_pk_mul_f32 v[236:237], v[236:237], v[232:233] op_sel_hi:[1,0]\n"
    "v_pk_mul_f32 v[238:239], v[238:239], v[232:233] op_sel_hi:[1,0]\n"
    "v_pk_mul_f32 v[240:241], v[240:241], v[232:233] op_sel_hi:[1,0]\n"
    "v_pk_mul_f32 v[242:243], v[242:243], v[232:233] op_sel_hi:[1,0]\n"
    "v_cvt_pk_bf16_f32 v236, v236, v237\n"
    "v_cvt_pk_bf16_f32 v237, v238, v239\n"
    "v_cvt_pk_bf16_f32 v238, v240, v241\n"
    "v_cvt_pk_bf16_f32 v239, v242, v243\n"
    "buffer_store_dwordx4 v[236:239], v1, s[44:47], s90 offen sc1\n"
    "ds_read_b64_tr_b16 v[72:73], v18 offset:4096\n"
    "ds_read_b64_tr_b16 v[74:75], v18 offset:6144\n"
    "ds_read_b64_tr_b16 v[76:77], v19 offset:4096\n"
    "ds_read_b64_tr_b16 v[78:79], v19 offset:6144\n"
    "ds_read_b64_tr_b16 v[80:81], v20 offset:4096\n"
    "ds_read_b64_tr_b16 v[82:83], v20 offset:6144\n"
    "s_waitcnt lgkmcnt(12)\n"
    "v_mfma_f32_16x16x32_bf16 v[88:91], v[56:59], v[152:155], 0\n"
    "ds_read_b64_tr_b16 v[84:85], v21 offset:4096\n"
    "ds_read_b64_tr_b16 v[86:87], v21 offset:6144\n"
    "v_mfma_f32_16x16x32_bf16 v[92:95], v[56:59], v[156:159], 0\n"
    "s_waitcnt lgkmcnt(0)\n"
    "s_barrier\n"
    "v_mfma_f32_16x16x32_bf16 v[100:103], v[60:63], v[156:159], 0\n"
    "ds_read_b64_tr_b16 v[136:137], v18 offset:8192\n"
    "ds_read_b64_tr_b16 v[138:139], v18 offset:10240\n"
    "v_mfma_f32_16x16x32_bf16 v[96:99], v[60:63], v[152:155], 0\n"
    "ds_read_b64_tr_b16 v[140:141], v19 offset:8192\n"
    "ds_read_b64_tr_b16 v[142:143], v19 offset:10240\n"
    "s_add_u32 m0, s88, 0\n"
    "s_add_u32 s61, s96, 196608\n"
    "buffer_load_dwordx4 v5, s[24:27], s61 offen lds\n"
    "v_mfma_f32_16x16x32_bf16 v[104:107], v[64:67], v[152:155], 0\n"
    "ds_read_b64_tr_b16 v[144:145], v20 offset:8192\n"
    "ds_read_b64_tr_b16 v[146:147], v20 offset:10240\n"
    "v_mfma_f32_16x16x32_bf16 v[108:111], v[64:67], v[156:159], 0\n"
    "ds_read_b64_tr_b16 v[148:149], v21 offset:8192\n"
    "ds_read_b64_tr_b16 v[150:151], v21 offset:10240\n"
    "s_add_u32 m0, s88, 1024\n"
    "s_add_u32 s61, s96, 204800\n"
    "buffer_load_dwordx4 v5, s[24:27], s61 offen lds\n"
    "v_mfma_f32_16x16x32_bf16 v[116:119], v[68:71], v[156:159], 0\n"
    "v_mfma_f32_16x16x32_bf16 v[112:115], v[68:71], v[152:155], 0\n"
    "s_add_u32 m0, s88, 4096\n"
    "s_add_u32 s61, s97, 0\n"
    "buffer_load_dwordx4 v5, s[24:27], s61 offen lds\n"
    "v_mfma_f32_16x16x32_bf16 v[88:91], v[72:75], v[160:163], v[88:91]\n"
    "ds_read_b64_tr_b16 v[56:57], v18 offset:12288\n"
    "ds_read_b64_tr_b16 v[58:59], v18 offset:14336\n"
    "v_mfma_f32_16x16x32_bf16 v[92:95], v[72:75], v[164:167], v[92:95]\n"
    "ds_read_b64_tr_b16 v[60:61], v19 offset:12288\n"
    "ds_read_b64_tr_b16 v[62:63], v19 offset:14336\n"
    "s_add_u32 m0, s88, 5120\n"
    "s_add_u32 s61, s97, 8192\n"
    "buffer_load_dwordx4 v5, s[24:27], s61 offen lds\n"
    "v_mfma_f32_16x16x32_bf16 v[100:103], v[76:79], v[164:167], v[100:103]\n"
    "ds_read_b64_tr_b16 v[64:65], v20 offset:12288\n"
    "ds_read_b64_tr_b16 v[66:67], v20 offset:14336\n"
    "v_mfma_f32_16x16x32_bf16 v[96:99], v[76:79], v[160:163], v[96:99]\n"
    "ds_read_b64_tr_b16 v[68:69], v21 offset:12288\n"
    "v_mfma_f32_16x16x32_bf16 v[104:107], v[80:83], v[160:163], v[104:107]\n"
    "v_mfma_f32_16x16x32_bf16 v[108:111], v[80:83], v[164:167], v[108:111]\n"
    "s_add_u32 m0, s88, 8192\n"
    "s_add_u32 s61, s97, 32768\n"
    "buffer_load_dwordx4 v5, s[24:27], s61 offen lds\n"
    "v_mfma_f32_16x16x32_bf16 v[116:119], v[84:87], v[164:167], v[116:119]\n"
    "v_mfma_f32_16x16x32_bf16 v[112:115], v[84:87], v[160:163], v[112:115]\n"
    "s_add_u32 m0, s88, 9216\n"
    "s_add_u32 s61, s97, 40960\n"
    "buffer_load_dwordx4 v5, s[24:27], s61 offen lds\n"
    "s_waitcnt lgkmcnt(13)\n"
    "v_mfma_f32_16x16x32_bf16 v[88:91], v[136:139], v[168:171], v[88:91]\n"
    "ds_read_b64_tr_b16 v[70:71], v21 offset:14336\n"
    "ds_read_b64_tr_b16 v[72:73], v18 offset:16384\n"
    "v_mfma_f32_16x16x32_bf16 v[92:95], v[136:139], v[172:175], v[92:95]\n"
    "s_add_u32 m0, s88, 12288\n"
    "s_add_u32 s61, s97, 65536\n"
    "buffer_load_dwordx4 v5, s[24:27], s61 offen lds\n"
    "s_waitcnt lgkmcnt(13)\n"
    "v_mfma_f32_16x16x32_bf16 v[100:103], v[140:143], v[172:175], v[100:103]\n"
    "ds_read_b64_tr_b16 v[74:75], v18 offset:18432\n"
    "ds_read_b64_tr_b16 v[76:77], v19 offset:16384\n"
    "v_mfma_f32_16x16x32_bf16 v[96:99], v[140:143], v[168:171], v[96:99]\n"
    "s_waitcnt lgkmcnt(13)\n"
    "v_mfma_f32_16x16x32_bf16 v[104:107], v[144:147], v[168:171], v[104:107]\n"
    "ds_read_b64_tr_b16 v[78:79], v19 offset:18432\n"
    "ds_read_b64_tr_b16 v[80:81], v20 offset:16384\n"
    "v_mfma_f32_16x16x32_bf16 v[108:111], v[144:147], v[172:175], v[108:111]\n"
    "s_add_u32 m0, s88, 13312\n"
    "s_add_u32 s61, s97, 73728\n"
    "buffer_load_dwordx4 v5, s[24:27], s61 offen lds\n"
    "s_waitcnt lgkmcnt(13)\n"
    "v_mfma_f32_16x16x32_bf16 v[116:119], v[148:151], v[172:175], v[116:119]\n"
    "ds_read_b64_tr_b16 v[82:83], v20 offset:18432\n"
    "ds_read_b64_tr_b16 v[84:85], v21 offset:16384\n"
    "v_mfma_f32_16x16x32_bf16 v[112:115], v[148:151], v[168:171], v[112:115]\n"
    "s_waitcnt lgkmcnt(13)\n"
    "v_mfma_f32_16x16x32_bf16 v[88:91], v[56:59], v[176:179], v[88:91]\n"
    "ds_read_b64_tr_b16 v[86:87], v21 offset:18432\n"
    "ds_read_b64_tr_b16 v[136:137], v18 offset:20480\n"
    "v_mfma_f32_16x16x32_bf16 v[92:95], v[56:59], v[180:183], v[92:95]\n"
    "s_add_u32 m0, s87, 0\n"
    "s_add_u32 s61, s97, 98304\n"
    "buffer_load_dwordx4 v5, s[24:27], s61 offen lds\n"
    "s_waitcnt lgkmcnt(13)\n"
    "v_mfma_f32_16x16x32_bf16 v[100:103], v[60:63], v[180:183], v[100:103]\n"
    "ds_read_b64_tr_b16 v[138:139], v18 offset:22528\n"
    "ds_read_b64_tr_b16 v[140:141], v19 offset:20480\n"
    "v_mfma_f32_16x16x32_bf16 v[96:99], v[60:63], v[176:179], v[96:99]\n"
    "s_waitcnt lgkmcnt(13)\n"
    "v_mfma_f32_16x16x32_bf16 v[104:107], v[64:67], v[176:179], v[104:107]\n"
    "ds_read_b64_tr_b16 v[142:143], v19 offset:22528\n"
    "ds_read_b64_tr_b16 v[144:145], v20 offset:20480\n"
    "v_mfma_f32_16x16x32_bf16 v[108:111], v[64:67], v[180:183], v[108:111]\n"
    "s_add_u32 m0, s87, 1024\n"
    "s_add_u32 s61, s97, 106496\n"
    "buffer_load_dwordx4 v5, s[24:27], s61 offen lds\n"
    "s_waitcnt lgkmcnt(13)\n"
    "v_mfma_f32_16x16x32_bf16 v[116:119], v[68:71], v[180:183], v[116:119]\n"
    "ds_read_b64_tr_b16 v[146:147], v20 offset:22528\n"
    "ds_read_b64_tr_b16 v[148:149], v21 offset:20480\n"
    "v_mfma_f32_16x16x32_bf16 v[112:115], v[68:71], v[176:179], v[112:115]\n"
    "s_waitcnt lgkmcnt(13)\n"
    "v_mfma_f32_16x16x32_bf16 v[88:91], v[72:75], v[184:187], v[88:91]\n"
    "ds_read_b64_tr_b16 v[150:151], v21 offset:22528\n"
    "v_mfma_f32_16x16x32_bf16 v[92:95], v[72:75], v[188:191], v[92:95]\n"
    "s_waitcnt lgkmcnt(0)\n"
    "s_waitcnt vmcnt(6)\n"
    "s_barrier\n"
    "v_mfma_f32_16x16x32_bf16 v[100:103], v[76:79], v[188:191], v[100:103]\n"
    "v_subrev_u32 v18, s95, v18\n"
    "ds_read_b64_tr_b16 v[56:57], v18 offset:0\n"
    "ds_read_b64_tr_b16 v[58:59], v18 offset:2048\n"
    "v_mfma_f32_16x16x32_bf16 v[96:99], v[76:79], v[184:187], v[96:99]\n"
    "v_subrev_u32 v19, s95, v19\n"
    "ds_read_b64_tr_b16 v[60:61], v19 offset:0\n"
    "ds_read_b64_tr_b16 v[62:63], v19 offset:2048\n"
    "v_mfma_f32_16x16x32_bf16 v[104:107], v[80:83], v[184:187], v[104:107]\n"
    "v_subrev_u32 v20, s95, v20\n"
    "ds_read_b64_tr_b16 v[64:65], v20 offset:0\n"
    "ds_read_b64_tr_b16 v[66:67], v20 offset:2048\n"
    "v_mfma_f32_16x16x32_bf16 v[108:111], v[80:83], v[188:191], v[108:111]\n"
    "v_subrev_u32 v21, s95, v21\n"
    "ds_read_b64_tr_b16 v[68:69], v21 offset:0\n"
    "ds_read_b64_tr_b16 v[70:71], v21 offset:2048\n"
    "s_add_u32 m0, s87, 4096\n"
    "s_add_u32 s61, s97, 131072\n"
    "buffer_load_dwordx4 v5, s[24:27], s61 offen lds\n"
    "v_mfma_f32_16x16x32_bf16 v[116:119], v[84:87], v[188:191], v[116:119]\n"
    "v_mfma_f32_16x16x32_bf16 v[112:115], v[84:87], v[184:187], v[112:115]\n"
    "v_mov_b64 v[10:11], s[22:23]\n"
    "v_mov_b64 v[12:13], s[22:23]\n"
    "s_mov_b64 exec, 0xff\n"
    "buffer_store_dwordx4 v[10:13], v1, s[44:47], s91 offen sc1\n"
    "s_mov_b64 exec, -1\n"
    "s_add_u32 m0, s87, 5120\n"
    "s_add_u32 s61, s97, 139264\n"
    "buffer_load_dwordx4 v5, s[24:27], s61 offen lds\n"
    "v_mfma_f32_16x16x32_bf16 v[88:91], v[136:139], v[192:195], v[88:91]\n"
    "ds_read_b64_tr_b16 v[72:73], v18 offset:4096\n"
    "ds_read_b64_tr_b16 v[74:75], v18 offset:6144\n"
    "v_mfma_f32_16x16x32_bf16 v[92:95], v[136:139], v[196:199], v[92:95]\n"
    "ds_read_b64_tr_b16 v[76:77], v19 offset:4096\n"
    "ds_read_b64_tr_b16 v[78:79], v19 offset:6144\n"
    "s_add_u32 m0, s87, 8192\n"
    "s_add_u32 s61, s97, 163840\n"
    "buffer_load_dwordx4 v5, s[24:27], s61 offen lds\n"
    "v_mfma_f32_16x16x32_bf16 v[100:103], v[140:143], v[196:199], v[100:103]\n"
    "ds_read_b64_tr_b16 v[80:81], v20 offset:4096\n"
    "ds_read_b64_tr_b16 v[82:83], v20 offset:6144\n"
    "v_mfma_f32_16x16x32_bf16 v[96:99], v[140:143], v[192:195], v[96:99]\n"
    "ds_read_b64_tr_b16 v[84:85], v21 offset:4096\n"
    "v_mfma_f32_16x16x32_bf16 v[104:107], v[144:147], v[192:195], v[104:107]\n"
    "v_mfma_f32_16x16x32_bf16 v[108:111], v[144:147], v[196:199], v[108:111]\n"
    "s_add_u32 m0, s87, 9216\n"
    "s_add_u32 s61, s97, 172032\n"
    "buffer_load_dwordx4 v5, s[24:27], s61 offen lds\n"
    "v_mfma_f32_16x16x32_bf16 v[116:119], v[148:151], v[196:199], v[116:119]\n"
    "v_mfma_f32_16x16x32_bf16 v[112:115], v[148:151], v[192:195], v[112:115]\n"
    "s_waitcnt lgkmcnt(13)\n"
    "v_mfma_f32_16x16x32_bf16 v[88:91], v[56:59], v[200:203], v[88:91]\n"
    "ds_read_b64_tr_b16 v[86:87], v21 offset:6144\n"
    "v_mfma_f32_16x16x32_bf16 v[92:95], v[56:59], v[204:207], v[92:95]\n"
    "s_waitcnt lgkmcnt(0)\n"
    "s_waitcnt vmcnt(7)\n"
    "s_barrier\n"
    "v_mfma_f32_16x16x32_bf16 v[100:103], v[60:63], v[204:207], v[100:103]\n"
    "ds_read_b64_tr_b16 v[136:137], v18 offset:8192\n"
    "ds_read_b64_tr_b16 v[138:139], v18 offset:10240\n"
    "v_mfma_f32_16x16x32_bf16 v[96:99], v[60:63], v[200:203], v[96:99]\n"
    "ds_read_b64_tr_b16 v[140:141], v19 offset:8192\n"
    "ds_read_b64_tr_b16 v[142:143], v19 offset:10240\n"
    "v_mfma_f32_16x16x32_bf16 v[104:107], v[64:67], v[200:203], v[104:107]\n"
    "ds_read_b64_tr_b16 v[144:145], v20 offset:8192\n"
    "ds_read_b64_tr_b16 v[146:147], v20 offset:10240\n"
    "v_mfma_f32_16x16x32_bf16 v[108:111], v[64:67], v[204:207], v[108:111]\n"
    "ds_read_b64_tr_b16 v[148:149], v21 offset:8192\n"
    "ds_read_b64_tr_b16 v[150:151], v21 offset:10240\n"
    "s_add_u32 m0, s87, 12288\n"
    "s_add_u32 s61, s97, 196608\n"
    "buffer_load_dwordx4 v5, s[24:27], s61 offen lds\n"
    "v_mfma_f32_16x16x32_bf16 v[116:119], v[68:71], v[204:207], v[116:119]\n"
    "v_mfma_f32_16x16x32_bf16 v[112:115], v[68:71], v[200:203], v[112:115]\n"
    "s_add_u32 m0, s87, 13312\n"
    "s_add_u32 s61, s97, 204800\n"
    "buffer_load_dwordx4 v5, s[24:27], s61 offen lds\n"
    "v_mfma_f32_16x16x32_bf16 v[88:91], v[72:75], v[208:211], v[88:91]\n"
    "ds_read_b64_tr_b16 v[56:57], v18 offset:12288\n"
    "ds_read_b64_tr_b16 v[58:59], v18 offset:14336\n"
    "v_mfma_f32_16x16x32_bf16 v[92:95], v[72:75], v[212:215], v[92:95]\n"
    "ds_read_b64_tr_b16 v[60:61], v19 offset:12288\n"
    "ds_read_b64_tr_b16 v[62:63], v19 offset:14336\n"
    "s_add_u32 m0, s87, 16384\n"
    "s_lshl_b32 s61, s80, 17\n"
    "s_add_u32 s61, s61, 0\n"
    "buffer_load_dwordx4 v5, s[28:31], s61 offen lds\n"
    "v_mfma_f32_16x16x32_bf16 v[100:103], v[76:79], v[212:215], v[100:103]\n"
    "ds_read_b64_tr_b16 v[64:65], v20 offset:12288\n"
    "ds_read_b64_tr_b16 v[66:67], v20 offset:14336\n"
    "v_mfma_f32_16x16x32_bf16 v[96:99], v[76:79], v[208:211], v[96:99]\n"
    "ds_read_b64_tr_b16 v[68:69], v21 offset:12288\n"
    "v_and_b32 v14, 7, v7\n"
    "v_lshlrev_b32 v14, 7, v14\n"
    "v_add_u32 v14, s92, v14\n"
    "buffer_load_dwordx2 v[8:9], v14, s[44:47], 0 offen sc1\n"
    "v_mfma_f32_16x16x32_bf16 v[104:107], v[80:83], v[208:211], v[104:107]\n"
    "v_mfma_f32_16x16x32_bf16 v[108:111], v[80:83], v[212:215], v[108:111]\n"
    "s_add_u32 m0, s87, 17408\n"
    "s_lshl_b32 s61, s80, 17\n"
    "s_add_u32 s61, s61, 8192\n"
    "buffer_load_dwordx4 v5, s[28:31], s61 offen lds\n"
    "v_mfma_f32_16x16x32_bf16 v[116:119], v[84:87], v[212:215], v[116:119]\n"
    "v_mfma_f32_16x16x32_bf16 v[112:115], v[84:87], v[208:211], v[112:115]\n"
    "s_waitcnt lgkmcnt(13)\n"
    "v_mfma_f32_16x16x32_bf16 v[88:91], v[136:139], v[216:219], v[88:91]\n"
    "ds_read_b64_tr_b16 v[70:71], v21 offset:14336\n"
    "v_mfma_f32_16x16x32_bf16 v[92:95], v[136:139], v[220:223], v[92:95]\n"
    "s_waitcnt lgkmcnt(0)\n"
    "s_waitcnt vmcnt(7)\n"
    "s_barrier\n"
    "v_mfma_f32_16x16x32_bf16 v[100:103], v[140:143], v[220:223], v[100:103]\n"
    "v_add_u32 v18, s95, v18\n"
    "ds_read_b64_tr_b16 v[72:73], v18 offset:0\n"
    "ds_read_b64_tr_b16 v[74:75], v18 offset:2048\n"
    "v_mfma_f32_16x16x32_bf16 v[96:99], v[140:143], v[216:219], v[96:99]\n"
    "v_add_u32 v19, s95, v19\n"
    "ds_read_b64_tr_b16 v[76:77], v19 offset:0\n"
    "ds_read_b64_tr_b16 v[78:79], v19 offset:2048\n"
    "v_mfma_f32_16x16x32_bf16 v[104:107], v[144:147], v[216:219], v[104:107]\n"
    "v_add_u32 v20, s95, v20\n"
    "ds_read_b64_tr_b16 v[80:81], v20 offset:0\n"
    "ds_read_b64_tr_b16 v[82:83], v20 offset:2048\n"
    "v_mfma_f32_16x16x32_bf16 v[108:111], v[144:147], v[220:223], v[108:111]\n"
    "v_add_u32 v21, s95, v21\n"
    "ds_read_b64_tr_b16 v[84:85], v21 offset:0\n"
    "ds_read_b64_tr_b16 v[86:87], v21 offset:2048\n"
    "s_add_u32 m0, s87, 20480\n"
    "s_lshl_b32 s61, s80, 17\n"
    "s_add_u32 s61, s61, 32768\n"
    "buffer_load_dwordx4 v5, s[28:31], s61 offen lds\n"
    "v_mfma_f32_16x16x32_bf16 v[116:119], v[148:151], v[220:223], v[116:119]\n"
    "v_mfma_f32_16x16x32_bf16 v[112:115], v[148:151], v[216:219], v[112:115]\n"
    "s_add_u32 m0, s87, 21504\n"
    "s_lshl_b32 s61, s80, 17\n"
    "s_add_u32 s61, s61, 40960\n"
    "buffer_load_dwordx4 v5, s[28:31], s61 offen lds\n"
    "v_mfma_f32_16x16x32_bf16 v[88:91], v[56:59], v[224:227], v[88:91]\n"
    "ds_read_b64_tr_b16 v[136:137], v18 offset:4096\n"
    "ds_read_b64_tr_b16 v[138:139], v18 offset:6144\n"
    "v_mfma_f32_16x16x32_bf16 v[92:95], v[56:59], v[228:231], v[92:95]\n"
    "ds_read_b64_tr_b16 v[140:141], v19 offset:4096\n"
    "ds_read_b64_tr_b16 v[142:143], v19 offset:6144\n"
    "s_add_u32 m0, s88, 0\n"
    "s_lshl_b32 s61, s80, 17\n"
    "s_add_u32 s61, s61, 65536\n"
    "buffer_load_dwordx4 v5, s[28:31], s61 offen lds\n"
    "v_mfma_f32_16x16x32_bf16 v[100:103], v[60:63], v[228:231], v[100:103]\n"
    "ds_read_b64_tr_b16 v[144:145], v20 offset:4096\n"
    "ds_read_b64_tr_b16 v[146:147], v20 offset:6144\n"
    "v_mfma_f32_16x16x32_bf16 v[96:99], v[60:63], v[224:227], v[96:99]\n"
    "ds_read_b64_tr_b16 v[148:149], v21 offset:4096\n"
    "v_mfma_f32_16x16x32_bf16 v[104:107], v[64:67], v[224:227], v[104:107]\n"
    "v_mfma_f32_16x16x32_bf16 v[108:111], v[64:67], v[228:231], v[108:111]\n"
    "s_add_u32 m0, s88, 1024\n"
    "s_lshl_b32 s61, s80, 17\n"
    "s_add_u32 s61, s61, 73728\n"
    "buffer_load_dwordx4 v5, s[28:31], s61 offen lds\n"
    "v_mfma_f32_16x16x32_bf16 v[116:119], v[68:71], v[228:231], v[116:119]\n"
    "v_mfma_f32_16x16x32_bf16 v[112:115], v[68:71], v[224:227], v[112:115]\n"
    "s_waitcnt lgkmcnt(13)\n"
    "v_mfma_f32_16x16x32_bf16 v[88:91], v[72:75], v[24:27], v[88:91]\n"
    "ds_read_b64_tr_b16 v[150:151], v21 offset:6144\n"
    "v_mfma_f32_16x16x32_bf16 v[92:95], v[72:75], v[28:31], v[92:95]\n"
    "s_waitcnt lgkmcnt(0)\n"
    "s_waitcnt vmcnt(7)\n"
    "s_barrier\n"
    "v_mfma_f32_16x16x32_bf16 v[100:103], v[76:79], v[28:31], v[100:103]\n"
    "ds_read_b64_tr_b16 v[56:57], v18 offset:8192\n"
    "ds_read_b64_tr_b16 v[58:59], v18 offset:10240\n"
    "v_mfma_f32_16x16x32_bf16 v[96:99], v[76:79], v[24:27], v[96:99]\n"
    "ds_read_b64_tr_b16 v[60:61], v19 offset:8192\n"
    "ds_read_b64_tr_b16 v[62:63], v19 offset:10240\n"
    "v_mfma_f32_16x16x32_bf16 v[104:107], v[80:83], v[24:27], v[104:107]\n"
    "ds_read_b64_tr_b16 v[64:65], v20 offset:8192\n"
    "ds_read_b64_tr_b16 v[66:67], v20 offset:10240\n"
    "v_mfma_f32_16x16x32_bf16 v[108:111], v[80:83], v[28:31], v[108:111]\n"
    "ds_read_b64_tr_b16 v[68:69], v21 offset:8192\n"
    "ds_read_b64_tr_b16 v[70:71], v21 offset:10240\n"
    "s_add_u32 m0, s88, 4096\n"
    "s_lshl_b32 s61, s80, 17\n"
    "s_add_u32 s61, s61, 98304\n"
    "buffer_load_dwordx4 v5, s[28:31], s61 offen lds\n"
    "v_mfma_f32_16x16x32_bf16 v[116:119], v[84:87], v[28:31], v[116:119]\n"
    "v_mfma_f32_16x16x32_bf16 v[112:115], v[84:87], v[24:27], v[112:115]\n"
    "s_waitcnt vmcnt(6)\n"
    "s_mov_b32 s66, 65536\n"
    "s_branch Lchk_34_%=\n"
    "Lpoll_32_%=:\n"
    "buffer_load_dwordx2 v[8:9], v14, s[44:47], 0 offen sc1\n"
    "s_waitcnt vmcnt(0)\n"
    "Lchk_34_%=:\n"
    "v_cmp_ne_u64 vcc, s[22:23], v[8:9]\n"
    "s_cbranch_vccz Ldone_33_%=\n"
    "s_sleep 3\n"
    "s_sub_u32 s66, s66, 1\n"
    "s_cmp_lg_u32 s66, 0\n"
    "s_cbranch_scc1 Lpoll_32_%=\n"
    "Ldone_33_%=:\n"
    "s_add_u32 s62, s93, 4096\n"
    "s_add_u32 m0, s88, 5120\n"
    "s_lshl_b32 s61, s80, 17\n"
    "s_add_u32 s61, s61, 106496\n"
    "buffer_load_dwordx4 v5, s[28:31], s61 offen lds\n"
    "v_mfma_f32_16x16x32_bf16 v[88:91], v[136:139], v[32:35], v[88:91]\n"
    "ds_read_b64_tr_b16 v[72:73], v18 offset:12288\n"
    "ds_read_b64_tr_b16 v[74:75], v18 offset:14336\n"
    "s_and_b32 s63, s87, 0xfffff000\n"
    "s_lshl_b32 s64, s16, 13\n"
    "v_mfma_f32_16x16x32_bf16 v[92:95], v[136:139], v[36:39], v[92:95]\n"
    "ds_read_b64_tr_b16 v[76:77], v19 offset:12288\n"
    "ds_read_b64_tr_b16 v[78:79], v19 offset:14336\n"
    "s_add_u32 s63, s63, s64\n"
    "v_bfe_u32 v12, v7, 2, 2\n"
    "buffer_load_dwordx4 v[152:155], v17, s[44:47], s93 offen offset:0 sc1\n"
    "v_mfma_f32_16x16x32_bf16 v[100:103], v[140:143], v[36:39], v[100:103]\n"
    "ds_read_b64_tr_b16 v[80:81], v20 offset:12288\n"
    "ds_read_b64_tr_b16 v[82:83], v20 offset:14336\n"
    "v_lshrrev_b32 v13, 5, v7\n"
    "v_add_u32 v12, v12, v13\n"
    "v_mfma_f32_16x16x32_bf16 v[96:99], v[140:143], v[32:35], v[96:99]\n"
    "ds_read_b64_tr_b16 v[84:85], v21 offset:12288\n"
    "v_and_b32 v12, 3, v12\n"
    "v_mfma_f32_16x16x32_bf16 v[104:107], v[144:147], v[32:35], v[104:107]\n"
    "v_and_b32 v13, 0x33, v7\n"
    "v_mfma_f32_16x16x32_bf16 v[108:111], v[144:147], v[36:39], v[108:111]\n"
    "v_lshl_or_b32 v12, v12, 2, v13\n"
    "buffer_load_dwordx4 v[156:159], v17, s[44:47], s93 offen offset:256 sc1\n"
    "v_mfma_f32_16x16x32_bf16 v[116:119], v[148:151], v[36:39], v[116:119]\n"
    "v_lshl_add_u32 v244, v12, 4, s63\n"
    "v_mfma_f32_16x16x32_bf16 v[112:115], v[148:151], v[32:35], v[112:115]\n"
    "v_xor_b32 v247, 0x80, v244\n"
    "s_waitcnt lgkmcnt(13)\n"
    "v_mfma_f32_16x16x32_bf16 v[88:91], v[56:59], v[40:43], v[88:91]\n"
    "ds_read_b64_tr_b16 v[86:87], v21 offset:14336\n"
    "s_lshr_b32 s63, s80, 1\n"
    "s_lshl_b32 s63, s63, 10\n"
    "v_mfma_f32_16x16x32_bf16 v[92:95], v[56:59], v[44:47], v[92:95]\n"
    "s_add_u32 s64, s64, s63\n"
    "s_add_u32 s64, s64, 65536\n"
    "s_waitcnt lgkmcnt(0)\n"
    "s_waitcnt vmcnt(6)\n"
    "s_barrier\n"
    "v_mfma_f32_16x16x32_bf16 v[100:103], v[60:63], v[44:47], v[100:103]\n"
    "v_add_u32 v18, v22, v18\n"
    "ds_read_b64_tr_b16 v[168:169], v18 offset:16384\n"
    "ds_read_b64_tr_b16 v[170:171], v18 offset:17408\n"
    "s_lshl_b32 s63, s80, 1\n"
    "v_lshrrev_b32 v12, 5, v7\n"
    "v_mfma_f32_16x16x32_bf16 v[96:99], v[60:63], v[40:43], v[96:99]\n"
    "v_add_u32 v19, v22, v19\n"
    "ds_read_b64_tr_b16 v[172:173], v19 offset:16384\n"
    "ds_read_b64_tr_b16 v[174:175], v19 offset:17408\n"
    "v_add3_u32 v12, v12, v7, s63\n"
    "v_mfma_f32_16x16x32_bf16 v[104:107], v[64:67], v[40:43], v[104:107]\n"
    "v_add_u32 v20, v22, v20\n"
    "ds_read_b64_tr_b16 v[176:177], v20 offset:16384\n"
    "ds_read_b64_tr_b16 v[178:179], v20 offset:17408\n"
    "v_and_b32 v12, 3, v12\n"
    "v_mfma_f32_16x16x32_bf16 v[108:111], v[64:67], v[44:47], v[108:111]\n"
    "v_add_u32 v21, v22, v21\n"
    "ds_read_b64_tr_b16 v[180:181], v21 offset:16384\n"
    "ds_read_b64_tr_b16 v[182:183], v21 offset:17408\n"
    "v_bfe_u32 v13, v7, 3, 2\n"
    "buffer_load_dwordx4 v[160:163], v17, s[44:47], s93 offen offset:2048 sc1\n"
    "v_mfma_f32_16x16x32_bf16 v[116:119], v[68:71], v[44:47], v[116:119]\n"
    "v_lshl_or_b32 v12, v12, 2, v13\n"
    "v_mfma_f32_16x16x32_bf16 v[112:115], v[68:71], v[40:43], v[112:115]\n"
    "v_and_b32 v13, 1, v7\n"
    "buffer_load_dwordx4 v[164:167], v17, s[44:47], s93 offen offset:2304 sc1\n"
    "v_mfma_f32_16x16x32_bf16 v[88:91], v[72:75], v[48:51], v[88:91]\n"
    "ds_read_b64_tr_b16 v[56:57], v18 offset:20480\n"
    "ds_read_b64_tr_b16 v[58:59], v18 offset:21504\n"
    "v_lshl_or_b32 v12, v13, 5, v12\n"
    "v_bfe_u32 v13, v7, 1, 2\n"
    "v_mfma_f32_16x16x32_bf16 v[92:95], v[72:75], v[52:55], v[92:95]\n"
    "ds_read_b64_tr_b16 v[60:61], v19 offset:20480\n"
    "ds_read_b64_tr_b16 v[62:63], v19 offset:21504\n"
    "v_lshl_or_b32 v12, v13, 7, v12\n"
    "v_lshl_add_u32 v245, v12, 4, s64\n"
    "buffer_load_dwordx4 v[232:235], v17, s[44:47], s62 offen offset:0 sc1\n"
    "v_mfma_f32_16x16x32_bf16 v[100:103], v[76:79], v[52:55], v[100:103]\n"
    "ds_read_b64_tr_b16 v[64:65], v20 offset:20480\n"
    "ds_read_b64_tr_b16 v[66:67], v20 offset:21504\n"
    "v_add_u32 v252, 49152, v245\n"
    "v_lshrrev_b32 v12, 3, v7\n"
    "v_mfma_f32_16x16x32_bf16 v[96:99], v[76:79], v[48:51], v[96:99]\n"
    "ds_read_b64_tr_b16 v[68:69], v21 offset:20480\n"
    "v_lshlrev_b32 v246, 10, v12\n"
    "v_mfma_f32_16x16x32_bf16 v[104:107], v[80:83], v[48:51], v[104:107]\n"
    "v_and_b32 v12, 7, v7\n"
    "v_mfma_f32_16x16x32_bf16 v[108:111], v[80:83], v[52:55], v[108:111]\n"
    "v_lshl_or_b32 v246, v12, 4, v246\n"
    "buffer_load_dwordx4 v[236:239], v17, s[44:47], s62 offen offset:256 sc1\n"
    "v_mfma_f32_16x16x32_bf16 v[116:119], v[84:87], v[52:55], v[116:119]\n"
    "v_sub_f32 v6, 1.0, v6\n"
    "v_mfma_f32_16x16x32_bf16 v[112:115], v[84:87], v[48:51], v[112:115]\n"
    "v_sub_f32 v16, 1.0, v16\n"
    "s_waitcnt vmcnt(4)\n"
    "s_waitcnt lgkmcnt(13)\n"
    "v_mfma_f32_16x16x32_bf16 v[120:123], v[168:171], v[152:155], 0\n"
    "ds_read_b64_tr_b16 v[70:71], v21 offset:21504\n"
    "v_mfma_f32_16x16x32_bf16 v[124:127], v[168:171], v[156:159], 0\n"
    "s_waitcnt lgkmcnt(0)\n"
    "s_barrier\n"
    "v_mfma_f32_16x16x32_bf16 v[132:135], v[172:175], v[156:159], 0\n"
    "v_subrev_u32 v18, s95, v18\n"
    "ds_read_b64_tr_b16 v[72:73], v18 offset:0\n"
    "ds_read_b64_tr_b16 v[74:75], v18 offset:1024\n"
    "v_mfma_f32_16x16x32_bf16 v[128:131], v[172:175], v[152:155], 0\n"
    "v_subrev_u32 v19, s95, v19\n"
    "ds_read_b64_tr_b16 v[76:77], v19 offset:0\n"
    "ds_read_b64_tr_b16 v[78:79], v19 offset:1024\n"
    "v_mfma_f32_16x16x32_bf16 v[136:139], v[176:179], v[152:155], 0\n"
    "v_subrev_u32 v20, s95, v20\n"
    "ds_read_b64_tr_b16 v[80:81], v20 offset:0\n"
    "ds_read_b64_tr_b16 v[82:83], v20 offset:1024\n"
    "v_mfma_f32_16x16x32_bf16 v[140:143], v[176:179], v[156:159], 0\n"
    "v_subrev_u32 v21, s95, v21\n"
    "ds_read_b64_tr_b16 v[84:85], v21 offset:0\n"
    "ds_read_b64_tr_b16 v[86:87], v21 offset:1024\n"
    "buffer_load_dwordx4 v[240:243], v17, s[44:47], s62 offen offset:2048 sc1\n"
    "v_mfma_f32_16x16x32_bf16 v[148:151], v[180:183], v[156:159], 0\n"
    "v_mfma_f32_16x16x32_bf16 v[144:147], v[180:183], v[152:155], 0\n"
    "buffer_load_dwordx4 v[248:251], v17, s[44:47], s62 offen offset:2304 sc1\n"
    "s_waitcnt vmcnt(4)\n"
    "v_mfma_f32_16x16x32_bf16 v[120:123], v[56:59], v[160:163], v[120:123]\n"
    "ds_read_b64_tr_b16 v[168:169], v18 offset:4096\n"
    "ds_read_b64_tr_b16 v[170:171], v18 offset:5120\n"
    "v_mfma_f32_16x16x32_bf16 v[124:127], v[56:59], v[164:167], v[124:127]\n"
    "ds_read_b64_tr_b16 v[172:173], v19 offset:4096\n"
    "ds_read_b64_tr_b16 v[174:175], v19 offset:5120\n"
    "v_mfma_f32_16x16x32_bf16 v[132:135], v[60:63], v[164:167], v[132:135]\n"
    "ds_read_b64_tr_b16 v[176:177], v20 offset:4096\n"
    "ds_read_b64_tr_b16 v[178:179], v20 offset:5120\n"
    "v_mfma_f32_16x16x32_bf16 v[128:131], v[60:63], v[160:163], v[128:131]\n"
    "ds_read_b64_tr_b16 v[180:181], v21 offset:4096\n"
    "v_mfma_f32_16x16x32_bf16 v[136:139], v[64:67], v[160:163], v[136:139]\n"
    "v_mfma_f32_16x16x32_bf16 v[140:143], v[64:67], v[164:167], v[140:143]\n"
    "v_mfma_f32_16x16x32_bf16 v[148:151], v[68:71], v[164:167], v[148:151]\n"
    "v_mfma_f32_16x16x32_bf16 v[144:147], v[68:71], v[160:163], v[144:147]\n"
    "s_waitcnt vmcnt(2)\n"
    "s_waitcnt lgkmcnt(13)\n"
    "v_mfma_f32_16x16x32_bf16 v[120:123], v[72:75], v[232:235], v[120:123]\n"
    "ds_read_b64_tr_b16 v[182:183], v21 offset:5120\n"
    "v_mfma_f32_16x16x32_bf16 v[124:127], v[72:75], v[236:239], v[124:127]\n"
    "s_waitcnt lgkmcnt(12)\n"
    "v_mfma_f32_16x16x32_bf16 v[132:135], v[76:79], v[236:239], v[132:135]\n"
    "v_mfma_f32_16x16x32_bf16 v[128:131], v[76:79], v[232:235], v[128:131]\n"
    "s_waitcnt lgkmcnt(10)\n"
    "v_mfma_f32_16x16x32_bf16 v[136:139], v[80:83], v[232:235], v[136:139]\n"
    "v_mfma_f32_16x16x32_bf16 v[140:143], v[80:83], v[236:239], v[140:143]\n"
    "s_waitcnt lgkmcnt(8)\n"
    "v_mfma_f32_16x16x32_bf16 v[148:151], v[84:87], v[236:239], v[148:151]\n"
    "v_mfma_f32_16x16x32_bf16 v[144:147], v[84:87], v[232:235], v[144:147]\n"
    "s_waitcnt vmcnt(0)\n"
    "s_waitcnt lgkmcnt(6)\n"
    "v_mfma_f32_16x16x32_bf16 v[120:123], v[168:171], v[240:243], v[120:123]\n"
    "v_mfma_f32_16x16x32_bf16 v[124:127], v[168:171], v[248:251], v[124:127]\n"
    "s_waitcnt lgkmcnt(4)\n"
    "v_mfma_f32_16x16x32_bf16 v[132:135], v[172:175], v[248:251], v[132:135]\n"
    "v_mfma_f32_16x16x32_bf16 v[128:131], v[172:175], v[240:243], v[128:131]\n"
    "s_waitcnt lgkmcnt(2)\n"
    "v_mfma_f32_16x16x32_bf16 v[136:139], v[176:179], v[240:243], v[136:139]\n"
    "v_mfma_f32_16x16x32_bf16 v[140:143], v[176:179], v[248:251], v[140:143]\n"
    "s_waitcnt lgkmcnt(0)\n"
    "v_mfma_f32_16x16x32_bf16 v[148:151], v[180:183], v[248:251], v[148:151]\n"
    "v_mfma_f32_16x16x32_bf16 v[144:147], v[180:183], v[240:243], v[144:147]\n"
    "s_nop 3\n"
    "v_pk_fma_f32 v[88:89], v[88:89], v[6:7], v[120:121] op_sel_hi:[1,0,1]\n"
    "v_pk_fma_f32 v[90:91], v[90:91], v[6:7], v[122:123] op_sel_hi:[1,0,1]\n"
    "ds_write_b128 v244, v[88:91] offset:0\n"
    "v_pk_fma_f32 v[92:93], v[92:93], v[16:17], v[124:125] op_sel_hi:[1,0,1]\n"
    "v_pk_fma_f32 v[94:95], v[94:95], v[16:17], v[126:127] op_sel_hi:[1,0,1]\n"
    "ds_write_b128 v244, v[92:95] offset:1024\n"
    "v_pk_fma_f32 v[96:97], v[96:97], v[6:7], v[128:129] op_sel_hi:[1,0,1]\n"
    "v_pk_fma_f32 v[98:99], v[98:99], v[6:7], v[130:131] op_sel_hi:[1,0,1]\n"
    "ds_write_b128 v247, v[96:99] offset:2048\n"
    "v_pk_fma_f32 v[100:101], v[100:101], v[16:17], v[132:133] op_sel_hi:[1,0,1]\n"
    "v_pk_fma_f32 v[102:103], v[102:103], v[16:17], v[134:135] op_sel_hi:[1,0,1]\n"
    "ds_write_b128 v247, v[100:103] offset:3072\n"
    "v_pk_fma_f32 v[104:105], v[104:105], v[6:7], v[136:137] op_sel_hi:[1,0,1]\n"
    "v_pk_fma_f32 v[106:107], v[106:107], v[6:7], v[138:139] op_sel_hi:[1,0,1]\n"
    "ds_write_b128 v244, v[104:107] offset:4096\n"
    "v_pk_fma_f32 v[108:109], v[108:109], v[16:17], v[140:141] op_sel_hi:[1,0,1]\n"
    "v_pk_fma_f32 v[110:111], v[110:111], v[16:17], v[142:143] op_sel_hi:[1,0,1]\n"
    "ds_write_b128 v244, v[108:111] offset:5120\n"
    "v_pk_fma_f32 v[112:113], v[112:113], v[6:7], v[144:145] op_sel_hi:[1,0,1]\n"
    "v_pk_fma_f32 v[114:115], v[114:115], v[6:7], v[146:147] op_sel_hi:[1,0,1]\n"
    "ds_write_b128 v247, v[112:115] offset:6144\n"
    "v_pk_fma_f32 v[116:117], v[116:117], v[16:17], v[148:149] op_sel_hi:[1,0,1]\n"
    "v_pk_fma_f32 v[118:119], v[118:119], v[16:17], v[150:151] op_sel_hi:[1,0,1]\n"
    "ds_write_b128 v247, v[116:119] offset:7168\n"
    "s_waitcnt lgkmcnt(0)\n"
    "s_barrier\n"
    "ds_read_b128 v[120:123], v245 offset:0\n"
    "ds_read_b128 v[124:127], v245 offset:256\n"
    "ds_read_b128 v[128:131], v245 offset:24576\n"
    "ds_read_b128 v[132:135], v245 offset:24832\n"
    "ds_read_b128 v[136:139], v252 offset:0\n"
    "ds_read_b128 v[140:143], v252 offset:256\n"
    "ds_read_b128 v[144:147], v252 offset:24576\n"
    "ds_read_b128 v[148:151], v252 offset:24832\n"
    "s_waitcnt lgkmcnt(4)\n"
    "v_pk_add_f32 v[120:121], v[120:121], v[128:129]\n"
    "v_pk_add_f32 v[122:123], v[122:123], v[130:131]\n"
    "v_pk_add_f32 v[124:125], v[124:125], v[132:133]\n"
    "v_pk_add_f32 v[126:127], v[126:127], v[134:135]\n"
    "s_waitcnt lgkmcnt(2)\n"
    "v_pk_add_f32 v[120:121], v[120:121], v[136:137]\n"
    "v_pk_add_f32 v[122:123], v[122:123], v[138:139]\n"
    "v_pk_add_f32 v[124:125], v[124:125], v[140:141]\n"
    "v_pk_add_f32 v[126:127], v[126:127], v[142:143]\n"
    "s_waitcnt lgkmcnt(0)\n"
    "v_pk_add_f32 v[120:121], v[120:121], v[144:145]\n"
    "v_pk_add_f32 v[122:123], v[122:123], v[146:147]\n"
    "v_pk_add_f32 v[124:125], v[124:125], v[148:149]\n"
    "v_pk_add_f32 v[126:127], v[126:127], v[150:151]\n"
    "v_cvt_pk_bf16_f32 v56, v120, v121\n"
    "v_cvt_pk_bf16_f32 v57, v122, v123\n"
    "v_cvt_pk_bf16_f32 v58, v124, v125\n"
    "v_cvt_pk_bf16_f32 v59, v126, v127\n"
    "buffer_store_dwordx4 v[56:59], v246, s[36:39], s94 offen\n"
    "s_branch Lfin_35_%=\n"
    "LslowB_21_%=:\n"
    "s_mov_b32 s66, 65536\n"
    "Lpoll_36_%=:\n"
    "buffer_load_dwordx4 v[8:11], v14, s[44:47], 0 offen sc1\n"
    "s_waitcnt vmcnt(0)\n"
    "Lchk_38_%=:\n"
    "v_cmp_ne_u64 vcc, s[22:23], v[8:9]\n"
    "v_cmp_ne_u64 s[48:49], s[22:23], v[10:11]\n"
    "s_nop 1\n"
    "s_or_b64 vcc, vcc, s[48:49]\n"
    "s_cbranch_vccz Ldone_37_%=\n"
    "s_sleep 3\n"
    "s_sub_u32 s66, s66, 1\n"
    "s_cmp_lg_u32 s66, 0\n"
    "s_cbranch_scc1 Lpoll_36_%=\n"
    "Ldone_37_%=:\n"
    "s_branch LbackB_22_%=\n"
    "Lepoch_23_%=:\n"
    "v_lshlrev_b32 v14, 7, v7\n"
    "v_add_u32 v14, 655360, v14\n"
    "s_mov_b32 s66, 65536\n"
    "Lpoll_39_%=:\n"
    "buffer_load_dwordx2 v[8:9], v14, s[44:47], 0 offen sc1\n"
    "s_waitcnt vmcnt(0)\n"
    "Lchk_41_%=:\n"
    "v_cmp_ne_u64 vcc, s[22:23], v[8:9]\n"
    "s_cbranch_vccz Ldone_40_%=\n"
    "s_sleep 3\n"
    "s_sub_u32 s66, s66, 1\n"
    "s_cmp_lg_u32 s66, 0\n"
    "s_cbranch_scc1 Lpoll_39_%=\n"
    "Ldone_40_%=:\n"
    "v_lshlrev_b32 v14, 7, v7\n"
    "v_add_u32 v14, 663552, v14\n"
    "s_mov_b32 s66, 65536\n"
    "Lpoll_42_%=:\n"
    "buffer_load_dwordx2 v[8:9], v14, s[44:47], 0 offen sc1\n"
    "s_waitcnt vmcnt(0)\n"
    "Lchk_44_%=:\n"
    "v_cmp_ne_u64 vcc, s[22:23], v[8:9]\n"
    "s_cbranch_vccz Ldone_43_%=\n"
    "s_sleep 3\n"
    "s_sub_u32 s66, s66, 1\n"
    "s_cmp_lg_u32 s66, 0\n"
    "s_cbranch_scc1 Lpoll_42_%=\n"
    "Ldone_43_%=:\n"
    "v_lshlrev_b32 v14, 7, v7\n"
    "v_add_u32 v14, 671744, v14\n"
    "s_mov_b32 s66, 65536\n"
    "Lpoll_45_%=:\n"
    "buffer_load_dwordx2 v[8:9], v14, s[44:47], 0 offen sc1\n"
    "s_waitcnt vmcnt(0)\n"
    "Lchk_47_%=:\n"
    "v_cmp_ne_u64 vcc, s[22:23], v[8:9]\n"
    "s_cbranch_vccz Ldone_46_%=\n"
    "s_sleep 3\n"
    "s_sub_u32 s66, s66, 1\n"
    "s_cmp_lg_u32 s66, 0\n"
    "s_cbranch_scc1 Lpoll_45_%=\n"
    "Ldone_46_%=:\n"
    "v_lshlrev_b32 v14, 7, v7\n"
    "v_add_u32 v14, 679936, v14\n"
    "s_mov_b32 s66, 65536\n"
    "Lpoll_48_%=:\n"
    "buffer_load_dwordx2 v[8:9], v14, s[44:47], 0 offen sc1\n"
    "s_waitcnt vmcnt(0)\n"
    "Lchk_50_%=:\n"
    "v_cmp_ne_u64 vcc, s[22:23], v[8:9]\n"
    "s_cbranch_vccz Ldone_49_%=\n"
    "s_sleep 3\n"
    "s_sub_u32 s66, s66, 1\n"
    "s_cmp_lg_u32 s66, 0\n"
    "s_cbranch_scc1 Lpoll_48_%=\n"
    "Ldone_49_%=:\n"
    "s_mov_b32 s61, 0\n"
    "v_mov_b64 v[10:11], s[22:23]\n"
    "s_mov_b64 exec, 1\n"
    "buffer_store_dwordx2 v[10:11], v1, s[44:47], s61 offen sc1\n"
    "s_mov_b64 exec, -1\n"
    "s_branch LepochB_24_%=\n"
    "Lfallback_30_%=:\n"
    "s_waitcnt vmcnt(0)\n"
    "buffer_load_dwordx4 v[136:139], v1, s[44:47], s89 offen sc1\n"
    "s_mov_b32 s78, 0\n"
    "v_mov_b32 v10, 0xff800000\n"
    "Lfa_51_%=:\n"
    "s_add_u32 s79, s76, s78\n"
    "buffer_load_dwordx4 v[140:143], v1, s[44:47], s79 offen sc1\n"
    "s_waitcnt vmcnt(0)\n"
    "v_mfma_f32_32x32x16_bf16 v[104:119], v[140:143], v[136:139], 0\n"
    "s_nop 15\n"
    "v_max3_f32 v10, v104, v105, v10\n"
    "v_max3_f32 v10, v106, v107, v10\n"
    "v_max3_f32 v10, v108, v109, v10\n"
    "v_max3_f32 v10, v110, v111, v10\n"
    "v_max3_f32 v10, v112, v113, v10\n"
    "v_max3_f32 v10, v114, v115, v10\n"
    "v_max3_f32 v10, v116, v117, v10\n"
    "v_max3_f32 v10, v118, v119, v10\n"
    "s_add_u32 s78, s78, 2048\n"
    "s_cmp_lt_u32 s78, 131072\n"
    "s_cbranch_scc1 Lfa_51_%=\n"
    "v_mov_b32 v11, v10\n"
    "s_nop 1\n"
    "v_permlane32_swap_b32 v11, v10\n"
    "s_nop 1\n"
    "v_max_f32 v10, v10, v11\n"
    "v_mov_b32 v144, v248\n"
    "v_mov_b32 v145, v248\n"
    "v_mov_b32 v146, v248\n"
    "v_mov_b32 v147, v248\n"
    "v_mov_b32 v148, v248\n"
    "v_mov_b32 v149, v248\n"
    "v_mov_b32 v150, v248\n"
    "v_mov_b32 v151, v248\n"
    "v_sub_f32 v120, 0, v10\n"
    "v_mov_b32 v88, 0\n"
    "v_sub_f32 v121, 0, v10\n"
    "v_mov_b32 v89, 0\n"
    "v_sub_f32 v122, 0, v10\n"
    "v_mov_b32 v90, 0\n"
    "v_sub_f32 v123, 0, v10\n"
    "v_mov_b32 v91, 0\n"
    "v_sub_f32 v124, 0, v10\n"
    "v_mov_b32 v92, 0\n"
    "v_sub_f32 v125, 0, v10\n"
    "v_mov_b32 v93, 0\n"
    "v_sub_f32 v126, 0, v10\n"
    "v_mov_b32 v94, 0\n"
    "v_sub_f32 v127, 0, v10\n"
    "v_mov_b32 v95, 0\n"
    "v_sub_f32 v128, 0, v10\n"
    "v_mov_b32 v96, 0\n"
    "v_sub_f32 v129, 0, v10\n"
    "v_mov_b32 v97, 0\n"
    "v_sub_f32 v130, 0, v10\n"
    "v_mov_b32 v98, 0\n"
    "v_sub_f32 v131, 0, v10\n"
    "v_mov_b32 v99, 0\n"
    "v_sub_f32 v132, 0, v10\n"
    "v_mov_b32 v100, 0\n"
    "v_sub_f32 v133, 0, v10\n"
    "v_mov_b32 v101, 0\n"
    "v_sub_f32 v134, 0, v10\n"
    "v_mov_b32 v102, 0\n"
    "v_sub_f32 v135, 0, v10\n"
    "v_mov_b32 v103, 0\n"
    "s_mov_b32 s78, 0\n"
    "Lfbb_52_%=:\n"
    "s_add_u32 s79, s76, s78\n"
    "buffer_load_dwordx4 v[140:143], v1, s[44:47], s79 offen sc1\n"
    "s_mov_b64 exec, s[34:35]\n"
    "buffer_load_dwordx4 v[144:147], v3, s[44:47], s79 offen offset:1024 sc1\n"
    "buffer_load_dwordx4 v[148:151], v3, s[44:47], s79 offen offset:1536 sc1\n"
    "s_mov_b64 exec, -1\n"
    "s_waitcnt vmcnt(0)\n"
    "v_mfma_f32_32x32x16_bf16 v[104:119], v[140:143], v[136:139], v[120:135]\n"
    "s_nop 15\n"
    "v_exp_f32 v104, v104\n"
    "v_exp_f32 v105, v105\n"
    "v_exp_f32 v106, v106\n"
    "v_exp_f32 v107, v107\n"
    "v_exp_f32 v108, v108\n"
    "v_exp_f32 v109, v109\n"
    "v_exp_f32 v110, v110\n"
    "v_exp_f32 v111, v111\n"
    "v_exp_f32 v112, v112\n"
    "v_exp_f32 v113, v113\n"
    "v_exp_f32 v114, v114\n"
    "v_exp_f32 v115, v115\n"
    "v_exp_f32 v116, v116\n"
    "v_exp_f32 v117, v117\n"
    "v_exp_f32 v118, v118\n"
    "v_exp_f32 v119, v119\n"
    "s_nop 3\n"
    "v_smfmac_f32_32x32x32_bf16 v[88:103], v[144:147], v[104:111], v4\n"
    "v_smfmac_f32_32x32x32_bf16 v[88:103], v[148:151], v[112:119], v4\n"
    "s_add_u32 s78, s78, 2048\n"
    "s_cmp_lt_u32 s78, 131072\n"
    "s_nop 15\n"
    "s_cbranch_scc1 Lfbb_52_%=\n"
    "v_mov_b32 v236, v88\n"
    "v_mov_b32 v237, v89\n"
    "v_mov_b32 v238, v90\n"
    "v_mov_b32 v239, v91\n"
    "v_mov_b32 v240, v92\n"
    "v_mov_b32 v241, v93\n"
    "v_mov_b32 v242, v94\n"
    "v_mov_b32 v243, v95\n"
    "v_mov_b32 v232, v96\n"
    "s_branch Lok_31_%=\n"
    "Lend_%=:\n"
    "Lfin_35_%=:\n"
    : "+s"(kargs), "+s"(ldsbase)
    : "s"(blockIdx.x), "v"(threadIdx.x)
    : "v1","v2","v3","v4","v5","v6","v7","v8","v9","v10","v11","v12","v13","v14","v15","v16","v17","v18","v19","v20","v21","v22","v23","v24","v25","v26","v27","v28","v29","v30","v31","v32","v33","v34","v35","v36","v37","v38","v39","v40","v41","v42","v43","v44","v45","v46","v47","v48","v49","v50","v51","v52","v53","v54","v55","v56","v57","v58","v59","v60","v61","v62","v63","v64","v65","v66","v67","v68","v69","v70","v71","v72","v73","v74","v75","v76","v77","v78","v79","v80","v81","v82","v83","v84","v85","v86","v87","v88","v89","v90","v91","v92","v93","v94","v95","v96","v97","v98","v99","v100","v101","v102","v103","v104","v105","v106","v107","v108","v109","v110","v111","v112","v113","v114","v115","v116","v117","v118","v119","v120","v121","v122","v123","v124","v125","v126","v127","v128","v129","v130","v131","v132","v133","v134","v135","v136","v137","v138","v139","v140","v141","v142","v143","v144","v145","v146","v147","v148","v149","v150","v151","v152","v153","v154","v155","v156","v157","v158","v159","v160","v161","v162","v163","v164","v165","v166","v167","v168","v169","v170","v171","v172","v173","v174","v175","v176","v177","v178","v179","v180","v181","v182","v183","v184","v185","v186","v187","v188","v189","v190","v191","v192","v193","v194","v195","v196","v197","v198","v199","v200","v201","v202","v203","v204","v205","v206","v207","v208","v209","v210","v211","v212","v213","v214","v215","v216","v217","v218","v219","v220","v221","v222","v223","v224","v225","v226","v227","v228","v229","v230","v231","v232","v233","v234","v235","v236","v237","v238","v239","v240","v241","v242","v243","v244","v245","v246","v247","v248","v249","v250","v251","v252","v253","v254","v255","s4","s5","s6","s7","s8","s9","s10","s11","s12","s13","s14","s15","s16","s17","s18","s19","s20","s21","s22","s23","s24","s25","s26","s27","s28","s29","s30","s31","s34","s35","s36","s37","s38","s39","s40","s41","s42","s43","s44","s45","s46","s47","s48","s49","s50","s51","s52","s53","s54","s55","s56","s57","s58","s59","s60","s61","s62","s63","s64","s65","s66","s67","s68","s69","s70","s71","s72","s73","s74","s75","s76","s77","s78","s79","s80","s81","s82","s83","s84","s85","s86","s87","s88","s89","s90","s91","s92","s93","s94","s95","s96","s97","s98","s99","vcc","scc","memory");
}

extern "C" void kernel_launch(void* const* d_in, const int* in_sizes, int n_in,
                              void* d_out, int out_size, void* d_ws, size_t ws_size,
                              hipStream_t stream) {
  gated_mix_fused_kernel<<<256, 512, 0, stream>>>(d_in[0], d_in[1], d_in[2], d_in[4], d_in[5], d_in[6], d_in[7], d_out, d_ws);
}
```
